```python
import torch

def _softmax(x, axis=-1):
    return torch.softmax(x, dim=axis)

def _gelu(x):
    return torch.nn.functional.gelu(x, approximate='tanh')

def _silu(x):
    return torch.nn.functional.silu(x)

def _sigmoid(x):
    return torch.sigmoid(x)

def _layernorm(x, axis=-1, eps=1e-05):
    m = x.mean(dim=axis, keepdim=True)
    v = x.var(dim=axis, keepdim=True, unbiased=False)
    return (x - m) / torch.sqrt(v + eps)

def _scatter_add(tgt, idx, src, axis=0):
    return tgt.clone().index_add_(axis, idx, src)

def _segment_sum(src, seg, n):
    out = torch.zeros((n,) + src.shape[1:], dtype=src.dtype)
    return out.index_add_(0, seg, src)

def _pad(x, pw):
    flat = tuple((v for pair in reversed(pw) for v in pair))
    return torch.nn.functional.pad(x, flat)
kernel_name = 'adaLN_cond__gqa_block_cached__glu_gated_reglu__decode'

def setup_inputs():

    _w = lambda a: (lambda t: t.to(torch.bfloat16) if t.is_floating_point() else t)(torch.as_tensor(a))

    def _A_setup():
        B, Sq, D = (8, 1, 1024)
        Hq, Hkv, Sk, Dh = (32, 4, 8192, 128)
        assert Hq % Hkv == 0, (Hq, Hkv)
        return {'x': _w((lambda *s: torch.randn(*s, dtype=torch.bfloat16) * 0.1)(B, Sq, D)), 'k_cache': _w((lambda *s: torch.randn(*s, dtype=torch.bfloat16) * 0.8)(B, Hkv, Sk, Dh)), 'v_cache': _w((lambda *s: torch.randn(*s, dtype=torch.bfloat16) * 3.2)(B, Hkv, Sk, Dh)), 'wq': _w((lambda *s: torch.randn(*s, dtype=torch.bfloat16) * 0.8)(D, Hq * Dh) * D ** (-0.5)), 'wo': _w((lambda *s: torch.randn(*s, dtype=torch.bfloat16) * 3.2)(Hq * Dh, D) * (Hq * Dh) ** (-0.5))}
    _A_in = _A_setup()

    def _B_setup():
        B, S, D, F = (8, 1, 1024, 3584)
        return {'x': _w((lambda *s: torch.randn(*s, dtype=torch.bfloat16) * 0.1)(B, S, D)), 'W_gate': _w((lambda *s: torch.randn(*s, dtype=torch.bfloat16) * 3.2)(D, F) * (2.0 / D) ** 0.5), 'W_up': _w((lambda *s: torch.randn(*s, dtype=torch.bfloat16) * 3.2)(D, F) * (2.0 / D) ** 0.5), 'W_down': _w((lambda *s: torch.randn(*s, dtype=torch.bfloat16) * 3.2)(F, D) * (1.0 / F) ** 0.5)}
    _B_in = _B_setup()
    return {'A_x': _A_in['x'], 'A_k_cache': _A_in['k_cache'], 'A_v_cache': _A_in['v_cache'], 'A_wq': _A_in['wq'], 'A_wo': _A_in['wo'], 'B_x': _B_in['x'], 'B_W_gate': _B_in['W_gate'], 'B_W_up': _B_in['W_up'], 'B_W_down': _B_in['W_down'], 'c': _w((lambda *s: torch.randn(*s, dtype=torch.bfloat16) * 0.4)(8, 1024) * 8.0), 'w_mod': _w((lambda *s: torch.randn(*s, dtype=torch.bfloat16) * 0.4)(1024, 6 * 1024) * 1024 ** (-0.5) * 8.0)}

def reference(A_x, A_k_cache, A_v_cache, A_wq, A_wo, B_x, B_W_gate, B_W_up, B_W_down, c, w_mod):

    def _A_ref(x, k_cache, v_cache, wq, wo):
        B, Sq, D = x.shape
        Hq, Dh = (32, 128)
        Hkv = k_cache.shape[1]
        rep = Hq // Hkv
        q = (lambda x, *d: x.permute(*d))((x @ wq).reshape(B, Sq, Hq, Dh), 0, 2, 1, 3)
        k = (lambda x, n, axis: torch.repeat_interleave(x, n, dim=axis))(k_cache, rep, axis=1)
        v = (lambda x, n, axis: torch.repeat_interleave(x, n, dim=axis))(v_cache, rep, axis=1)
        scores = torch.einsum('bhqd,bhkd->bhqk', q, k) * Dh ** (-0.5)
        p = _softmax(scores, axis=-1)
        o = torch.einsum('bhqk,bhkd->bhqd', p, v)
        return (lambda x, *d: x.permute(*d))(o, 0, 2, 1, 3).reshape(B, Sq, Hq * Dh) @ wo

    def _B_ref(x, W_gate, W_up, W_down):
        gate = torch.einsum('bsd,df->bsf', x, W_gate)
        up = torch.einsum('bsd,df->bsf', x, W_up)
        gate = gate * (lambda x, dt: x.to(dt))(gate > 0, gate.dtype)
        h = gate * up
        return torch.einsum('bsf,fd->bsd', h, W_down)
    x = A_x
    mod = (_silu(c) @ w_mod).reshape(c.shape[0], 6, 1, -1)
    sa, ca, ga, sb, cb, gb = (mod[:, 0].clone(), mod[:, 1].clone(), mod[:, 2].clone(), mod[:, 3].clone(), mod[:, 4].clone(), mod[:, 5].clone())
    h = _layernorm(x, axis=-1) * (1 + ca) + sa
    x = x + ga * _A_ref(h, A_k_cache, A_v_cache, A_wq, A_wo)
    h = _layernorm(x, axis=-1) * (1 + cb) + sb
    x = x + gb * _B_ref(h, B_W_gate, B_W_up, B_W_down)
    return x
```

```cpp
#include <hip/hip_runtime.h>
#include <hip/hip_bf16.h>
#include <stdint.h>

typedef unsigned short u16;
typedef unsigned v4u __attribute__((ext_vector_type(4)));
typedef float f32x4 __attribute__((ext_vector_type(4)));

__device__ __forceinline__ float bf2f(u16 v) { return __builtin_bit_cast(float, (unsigned)v << 16); }
__device__ __forceinline__ float bflo(unsigned v) { return __builtin_bit_cast(float, v << 16); }
__device__ __forceinline__ float bfhi(unsigned v) { return __builtin_bit_cast(float, v & 0xffff0000u); }
__device__ __forceinline__ u16 f2bf(float f) { __hip_bfloat16 b = __float2bfloat16(f); return __builtin_bit_cast(u16, b); }
__device__ __forceinline__ float rbf(float f) { return bf2f(f2bf(f)); }

constexpr int B = 8, D = 1024, HQ = 32, HKV = 4, SK = 8192, DH = 128, F = 3584;

template <int KS>
__device__ __forceinline__ void gemv_tile(const float* act  , const u16* W, int ldw, int n0, int k0, float* red  , float* outtile  ) {
  const int t = threadIdx.x, c = t & 7, r = t >> 3;
  float acc[8][8];
#pragma unroll
  for (int b = 0; b < 8; ++b)
#pragma unroll
    for (int j = 0; j < 8; ++j) acc[b][j] = 0.f;
#pragma unroll 4
  for (int k = r; k < KS; k += 32) {
    v4u w = __builtin_nontemporal_load((const v4u*)(W + (size_t)(k0 + k) * ldw + n0 + c * 8));
    float wf[8];
#pragma unroll
    for (int j = 0; j < 4; ++j) { wf[2 * j] = bflo(w[j]); wf[2 * j + 1] = bfhi(w[j]); }
    f32x4 a0 = *(const f32x4*)(act + k * 8), a1 = *(const f32x4*)(act + k * 8 + 4);
#pragma unroll
    for (int j = 0; j < 8; ++j) {
#pragma unroll
      for (int b = 0; b < 4; ++b) { acc[b][j] = fmaf(a0[b], wf[j], acc[b][j]); acc[b + 4][j] = fmaf(a1[b], wf[j], acc[b + 4][j]); }
    }
  }
#pragma unroll
  for (int b = 0; b < 8; ++b)
#pragma unroll
    for (int j = 0; j < 8; ++j) red[(r * 8 + b) * 64 + c * 8 + j] = acc[b][j];
  __syncthreads();
  for (int o = t; o < 512; o += 256) {
    float s = 0.f;
    for (int rr = 0; rr < 32; ++rr) s += red[rr * 512 + o];
    outtile[o] = s;
  }
  __syncthreads();
}

__global__ void __launch_bounds__(256) k_mod(const u16* c, const u16* wmod, float* modp  ) {
  __shared__ float act[256 * 8];
  __shared__ float red[32 * 512];
  __shared__ float ot[512];
  const int n0 = blockIdx.x * 64, k0 = blockIdx.y * 256;
  for (int i = threadIdx.x; i < 256 * 8; i += 256) {
    int k = i >> 3, b = i & 7;
    float v = bf2f(c[b * D + k0 + k]);
    act[i] = rbf(v / (1.f + __expf(-v)));
  }
  __syncthreads();
  gemv_tile<256>(act, wmod, 6 * D, n0, k0, red, ot);
  for (int o = threadIdx.x; o < 512; o += 256) modp[((size_t)blockIdx.y * 8 + (o >> 6)) * 6144 + n0 + (o & 63)] = ot[o];
}

__device__ __forceinline__ float modv(const float* modp, int b, int n) {
  float s = 0.f;
  for (int i = 0; i < 4; ++i) s += modp[((size_t)i * 8 + b) * 6144 + n];
  return rbf(s);
}

__device__ __forceinline__ void ln_stats(const float* xs, float* mean, float* rden) {
  const int w = threadIdx.x >> 6, lane = threadIdx.x & 63;
  for (int b = w; b < 8; b += 4) {
    float s = 0.f;
    for (int i = lane; i < D; i += 64) s += xs[b * D + i];
    for (int o = 32; o; o >>= 1) s += __shfl_xor(s, o);
    float m = s / D;
    float v = 0.f;
    for (int i = lane; i < D; i += 64) { float d = xs[b * D + i] - m; v += d * d; }
    for (int o = 32; o; o >>= 1) v += __shfl_xor(v, o);
    v /= D;
    if (lane == 0) { mean[b] = rbf(m); rden[b] = rbf(sqrtf(rbf(rbf(v) + 1e-5f))); }
  }
  __syncthreads();
}

__global__ void __launch_bounds__(256) k_q(const u16* x, const float* modp, const u16* wq, float* qp  ) {
  __shared__ float xs[8 * D];
  __shared__ float act[256 * 8];
  __shared__ float red[32 * 512];
  __shared__ float ot[512];
  __shared__ float mean[8], rden[8];
  const int n0 = blockIdx.x * 64, k0 = blockIdx.y * 256;
  for (int i = threadIdx.x; i < 8 * D; i += 256) xs[i] = bf2f(x[i]);
  __syncthreads();
  ln_stats(xs, mean, rden);
  for (int i = threadIdx.x; i < 256 * 8; i += 256) {
    int k = i >> 3, b = i & 7, d = k0 + k;
    float ln = rbf(rbf(xs[b * D + d] - mean[b]) / rden[b]);
    float sa = modv(modp, b, d), ca = modv(modp, b, D + d);
    act[i] = rbf(rbf(ln * rbf(1.f + ca)) + sa);
  }
  __syncthreads();
  gemv_tile<256>(act, wq, HQ * DH, n0, k0, red, ot);
  for (int o = threadIdx.x; o < 512; o += 256) qp[((size_t)blockIdx.y * 8 + (o >> 6)) * 4096 + n0 + (o & 63)] = ot[o];
}

constexpr int NS = 16, KPS = SK / NS;
__global__ void __launch_bounds__(256) k_attn(const float* qp, const u16* kc, const u16* vc, float* po  , float* pml  ) {
  __shared__ float qs[8 * DH];
  __shared__ float sc[8 * KPS];
  __shared__ float mx[8], ls[8];
  __shared__ float ored[16 * 8 * DH];
  const int s = blockIdx.x, kvh = blockIdx.y, b = blockIdx.z, t = threadIdx.x;
  for (int i = t; i < 8 * DH; i += 256) {
    float v = 0.f;
    for (int p = 0; p < 4; ++p) v += qp[((size_t)p * 8 + b) * 4096 + kvh * 1024 + i];
    qs[i] = rbf(v);
  }
  __syncthreads();
  const u16* kb = kc + ((size_t)(b * HKV + kvh) * SK + s * KPS) * DH;
  const u16* vb = vc + ((size_t)(b * HKV + kvh) * SK + s * KPS) * DH;
  const int c = t & 15, r = t >> 4;
  float q[8][8];
#pragma unroll
  for (int h = 0; h < 8; ++h)
#pragma unroll
    for (int j = 0; j < 8; ++j) q[h][j] = qs[h * DH + c * 8 + j];
#pragma unroll 4
  for (int k = r; k < KPS; k += 16) {
    v4u w = __builtin_nontemporal_load((const v4u*)(kb + (size_t)k * DH + c * 8));
    float wf[8];
#pragma unroll
    for (int j = 0; j < 4; ++j) { wf[2 * j] = bflo(w[j]); wf[2 * j + 1] = bfhi(w[j]); }
#pragma unroll
    for (int h = 0; h < 8; ++h) {
      float d = 0.f;
#pragma unroll
      for (int j = 0; j < 8; ++j) d = fmaf(q[h][j], wf[j], d);
      d += __shfl_xor(d, 1); d += __shfl_xor(d, 2); d += __shfl_xor(d, 4); d += __shfl_xor(d, 8);
      if (c == h) sc[h * KPS + k] = rbf(rbf(d) * 0.08838834764831845f);
    }
  }
  __syncthreads();
  {
    const int w = t >> 6, lane = t & 63;
    for (int h = w; h < 8; h += 4) {
      float m = -INFINITY;
      for (int i = lane; i < KPS; i += 64) m = fmaxf(m, sc[h * KPS + i]);
      for (int o = 32; o; o >>= 1) m = fmaxf(m, __shfl_xor(m, o));
      float l = 0.f;
      for (int i = lane; i < KPS; i += 64) { float p = __expf(sc[h * KPS + i] - m); sc[h * KPS + i] = p; l += p; }
      for (int o = 32; o; o >>= 1) l += __shfl_xor(l, o);
      if (lane == 0) { mx[h] = m; ls[h] = l; }
    }
  }
  __syncthreads();
  float acc[8][8];
#pragma unroll
  for (int h = 0; h < 8; ++h)
#pragma unroll
    for (int j = 0; j < 8; ++j) acc[h][j] = 0.f;
#pragma unroll 4
  for (int k = r; k < KPS; k += 16) {
    v4u w = __builtin_nontemporal_load((const v4u*)(vb + (size_t)k * DH + c * 8));
    float wf[8];
#pragma unroll
    for (int j = 0; j < 4; ++j) { wf[2 * j] = bflo(w[j]); wf[2 * j + 1] = bfhi(w[j]); }
#pragma unroll
    for (int h = 0; h < 8; ++h) {
      float p = sc[h * KPS + k];
#pragma unroll
      for (int j = 0; j < 8; ++j) acc[h][j] = fmaf(p, wf[j], acc[h][j]);
    }
  }
#pragma unroll
  for (int h = 0; h < 8; ++h)
#pragma unroll
    for (int j = 0; j < 8; ++j) ored[(r * 8 + h) * DH + c * 8 + j] = acc[h][j];
  __syncthreads();
  for (int o = t; o < 8 * DH; o += 256) {
    float v = 0.f;
    for (int rr = 0; rr < 16; ++rr) v += ored[rr * 8 * DH + o];
    int h = o >> 7, d = o & 127;
    po[(((size_t)b * HQ + kvh * 8 + h) * NS + s) * DH + d] = v;
  }
  if (t < 8) { pml[(((size_t)b * HQ + kvh * 8 + t) * NS + s) * 2] = mx[t]; pml[(((size_t)b * HQ + kvh * 8 + t) * NS + s) * 2 + 1] = ls[t]; }
}

__global__ void __launch_bounds__(256) k_wo(const float* po, const float* pml, const u16* wo, float* ap  ) {
  __shared__ float act[256 * 8];
  __shared__ float red[32 * 512];
  __shared__ float ot[512];
  const int n0 = blockIdx.x * 64, k0 = blockIdx.y * 256;
  for (int i = threadIdx.x; i < 256 * 8; i += 256) {
    int b = i >> 8, k = i & 255, kk = k0 + k, h = kk >> 7, d = kk & 127;
    const float* ml = pml + ((size_t)b * HQ + h) * NS * 2;
    float M = -INFINITY;
    for (int s = 0; s < NS; ++s) M = fmaxf(M, ml[2 * s]);
    float L = 0.f, o = 0.f;
    for (int s = 0; s < NS; ++s) { float w = __expf(ml[2 * s] - M); L += w * ml[2 * s + 1]; o += w * po[(((size_t)b * HQ + h) * NS + s) * DH + d]; }
    act[k * 8 + b] = rbf(o / L);
  }
  __syncthreads();
  gemv_tile<256>(act, wo, D, n0, k0, red, ot);
  for (int o = threadIdx.x; o < 512; o += 256) ap[((size_t)blockIdx.y * 8 + (o >> 6)) * D + n0 + (o & 63)] = ot[o];
}

__global__ void __launch_bounds__(256) k_gu(const u16* x, const float* modp, const float* ap, const u16* wg, const u16* wu, float* gp  , float* x1out) {
  __shared__ float xs[8 * D];
  __shared__ float act[512 * 8];
  __shared__ float red[32 * 512];
  __shared__ float ot[512];
  __shared__ float mean[8], rden[8];
  const int tile = blockIdx.x % 56, which = blockIdx.x / 56;
  const int n0 = tile * 64, k0 = blockIdx.y * 512;
  for (int i = threadIdx.x; i < 8 * D; i += 256) {
    int b = i >> 10, d = i & 1023;
    float a = 0.f;
    for (int p = 0; p < 16; ++p) a += ap[((size_t)p * 8 + b) * D + d];
    float ga = modv(modp, b, 2 * D + d);
    float v = rbf(bf2f(x[i]) + rbf(ga * rbf(a)));
    xs[i] = v;
    if (blockIdx.x == 0 && blockIdx.y == 0) x1out[i] = v;
  }
  __syncthreads();
  ln_stats(xs, mean, rden);
  for (int i = threadIdx.x; i < 512 * 8; i += 256) {
    int k = i >> 3, b = i & 7, d = k0 + k;
    float ln = rbf(rbf(xs[b * D + d] - mean[b]) / rden[b]);
    float sb = modv(modp, b, 3 * D + d), cb = modv(modp, b, 4 * D + d);
    act[i] = rbf(rbf(ln * rbf(1.f + cb)) + sb);
  }
  __syncthreads();
  gemv_tile<512>(act, which ? wu : wg, F, n0, k0, red, ot);
  for (int o = threadIdx.x; o < 512; o += 256) gp[(((size_t)blockIdx.y * 2 + which) * 8 + (o >> 6)) * F + n0 + (o & 63)] = ot[o];
}

__global__ void __launch_bounds__(256) k_down(const float* gp, const u16* wd, float* dp  ) {
  __shared__ float act[256 * 8];
  __shared__ float red[32 * 512];
  __shared__ float ot[512];
  const int n0 = blockIdx.x * 64, k0 = blockIdx.y * 256;
  for (int i = threadIdx.x; i < 256 * 8; i += 256) {
    int b = i >> 8, k = i & 255, f = k0 + k;
    float g = rbf(gp[((size_t)0 * 8 + b) * F + f] + gp[((size_t)2 * 8 + b) * F + f]);
    float u = rbf(gp[((size_t)1 * 8 + b) * F + f] + gp[((size_t)3 * 8 + b) * F + f]);
    g = g > 0.f ? g : g * 0.f;
    act[k * 8 + b] = rbf(g * u);
  }
  __syncthreads();
  gemv_tile<256>(act, wd, D, n0, k0, red, ot);
  for (int o = threadIdx.x; o < 512; o += 256) dp[((size_t)blockIdx.y * 8 + (o >> 6)) * D + n0 + (o & 63)] = ot[o];
}

__global__ void __launch_bounds__(256) k_out(const float* x1, const float* modp, const float* dp, u16* out) {
  int i = blockIdx.x * 256 + threadIdx.x;
  int b = i >> 10, d = i & 1023;
  float a = 0.f;
  for (int p = 0; p < 14; ++p) a += dp[((size_t)p * 8 + b) * D + d];
  float gb = modv(modp, b, 5 * D + d);
  out[i] = f2bf(x1[i] + rbf(gb * rbf(a)));
}

extern "C" void kernel_launch(void* const* d_in, const int* in_sizes, int n_in, void* d_out, int out_size, void* d_ws, size_t ws_size, hipStream_t stream) {
  const u16* x = (const u16*)d_in[0];
  const u16* kc = (const u16*)d_in[1];
  const u16* vc = (const u16*)d_in[2];
  const u16* wq = (const u16*)d_in[3];
  const u16* wo = (const u16*)d_in[4];
  const u16* wg = (const u16*)d_in[6];
  const u16* wu = (const u16*)d_in[7];
  const u16* wd = (const u16*)d_in[8];
  const u16* c = (const u16*)d_in[9];
  const u16* wmod = (const u16*)d_in[10];
  float* ws = (float*)d_ws;
  float* modp = ws;
  float* qp = modp + 4 * 8 * 6144;
  float* po = qp + 4 * 8 * 4096;
  float* pml = po + 8 * 32 * NS * 128;
  float* ap = pml + 8 * 32 * NS * 2;
  float* gp = ap + 16 * 8 * 1024;
  float* x1 = gp + 4 * 8 * F;
  float* dp = x1 + 8 * 1024;
  k_mod<<<dim3(96, 4), 256, 0, stream>>>(c, wmod, modp);
  k_q<<<dim3(64, 4), 256, 0, stream>>>(x, modp, wq, qp);
  k_attn<<<dim3(NS, HKV, B), 256, 0, stream>>>(qp, kc, vc, po, pml);
  k_wo<<<dim3(16, 16), 256, 0, stream>>>(po, pml, wo, ap);
  k_gu<<<dim3(112, 2), 256, 0, stream>>>(x, modp, ap, wg, wu, gp, x1);
  k_down<<<dim3(16, 14), 256, 0, stream>>>(gp, wd, dp);
  k_out<<<32, 256, 0, stream>>>(x1, modp, dp, (u16*)d_out);
}
```

```cpp
#include <hip/hip_runtime.h>
#include <stdint.h>

typedef unsigned short u16;
typedef unsigned long long u64;
typedef unsigned v4u __attribute__((ext_vector_type(4)));
typedef unsigned v2u __attribute__((ext_vector_type(2)));
typedef float f32x4 __attribute__((ext_vector_type(4)));
typedef float f32x2 __attribute__((ext_vector_type(2)));
typedef __bf16 bf16x8 __attribute__((ext_vector_type(8)));
typedef __bf16 bf16x2 __attribute__((ext_vector_type(2)));
typedef __amdgpu_buffer_rsrc_t rsrc_t;
#define DI __device__ __forceinline__
#define SB0() __builtin_amdgcn_sched_barrier(0)
#define CBAR() asm volatile("" ::: "memory")

#define STAMPS 0
#define T4LATE 0
#define KWIN 3

constexpr int WS_EPOCH = 0;
constexpr int WS_FLAGS = 1024, FLAG_REP = 2304;
constexpr int WS_MPA = 32768, MPA_SLOT = 2304;
constexpr int WS_ARENA = 1 << 20;
constexpr int WS_QP = WS_ARENA;
constexpr int WS_OP = WS_QP + (512 << 10);
constexpr int WS_XP = WS_OP + (512 << 10);
constexpr int WS_MPB = WS_XP + (512 << 10);
constexpr int WS_DP = WS_MPB + (512 << 10);
constexpr int REP_STRIDE = 16384 + 256;
constexpr int REPX_STRIDE = 16384 + 2048 + 256;
constexpr int WS_X1 = WS_DP + (256 << 10);
constexpr int WS_H = WS_X1 + 8 * REPX_STRIDE;
constexpr int WS_GU = WS_H + 8 * REP_STRIDE;
constexpr int WS_SB = WS_GU + (112 << 10);
constexpr int WS_ML = WS_SB + (32 << 10);
constexpr int WS_END = WS_ML + (16 << 10);
constexpr int ARENA_PER = 11 * 1024;
static_assert(WS_END <= WS_ARENA + 256 * ARENA_PER, "arena");
constexpr int WS_BYTES = 4 << 20;
constexpr unsigned SENT = 0xFFFFFFFFu;
#define CAPSH 0
constexpr int GCAP = 1 << (15 - CAPSH);
constexpr int LCAP = 1 << (22 - CAPSH);

enum { C_RESET = 0, C_GO, C_M1, C_Q, C_QG, C_MLW, C_MLF, C_M2, C_O, C_T1, C_T2, C_H2, C_T3, C_T4, C_N = 32 };

DI float bflo(unsigned v) { return __builtin_bit_cast(float, v << 16); }
DI float bfhi(unsigned v) { return __builtin_bit_cast(float, v & 0xffff0000u); }
DI unsigned pk2(float a, float b) { bf16x2 v = {(__bf16)a, (__bf16)b}; unsigned r = __builtin_bit_cast(unsigned, v); asm("" : "+v"(r)); return r; }
DI float rbf(float a) { return (float)(__bf16)a; }
DI unsigned canon(unsigned u) { return u < 0xFFFFFFFEu ? u : 0xFFFFFFFEu; }
DI unsigned fbits(float f) { return __builtin_bit_cast(unsigned, f); }
DI float bitsf(unsigned u) { return __builtin_bit_cast(float, u); }
DI rsrc_t mk(const void* p, unsigned bytes) { return __builtin_amdgcn_make_buffer_rsrc((void*)p, 0, bytes, 0x00020000); }
DI v4u ld_nt(rsrc_t r, int v, int s) { return __builtin_amdgcn_raw_buffer_load_b128(r, v, s, 2); }
DI v4u ld_pl(rsrc_t r, int v, int s) { return __builtin_amdgcn_raw_buffer_load_b128(r, v, s, 0); }
DI v2u ld_pl2(rsrc_t r, int v, int s) { return __builtin_amdgcn_raw_buffer_load_b64(r, v, s, 0); }
DI v4u ld_sc(rsrc_t r, int v, int s) { return __builtin_amdgcn_raw_buffer_load_b128(r, v, s, 16); }
DI v2u ld_sc2(rsrc_t r, int v, int s) { return __builtin_amdgcn_raw_buffer_load_b64(r, v, s, 16); }
DI void st_sc(v4u x, rsrc_t r, int v, int s) { __builtin_amdgcn_raw_buffer_store_b128(x, r, v, s, 16); }
DI void st_sc2(v2u x, rsrc_t r, int v, int s) { __builtin_amdgcn_raw_buffer_store_b64(x, r, v, s, 16); }
DI f32x4 mfma(v4u a, v4u b, f32x4 c) {
  return __builtin_amdgcn_mfma_f32_16x16x32_bf16(__builtin_bit_cast(bf16x8, a), __builtin_bit_cast(bf16x8, b), c, 0, 0, 0);
}
template <int CTRL> DI unsigned dppu(unsigned x) { return __builtin_amdgcn_update_dpp(0u, x, CTRL, 0xF, 0xF, true); }
template <int CTRL> DI float dppf(float x) { return bitsf(dppu<CTRL>(fbits(x))); }
DI float add8(float x) { return x + dppf<0x128>(x); }
DI float add16(float x) { auto s = __builtin_amdgcn_permlane16_swap(fbits(x), fbits(x), false, false); return bitsf(s[0]) + bitsf(s[1]); }
DI float add32(float x) { auto s = __builtin_amdgcn_permlane32_swap(fbits(x), fbits(x), false, false); return bitsf(s[0]) + bitsf(s[1]); }
DI float max8(float x) { return fmaxf(x, dppf<0x128>(x)); }
DI float max16(float x) { auto s = __builtin_amdgcn_permlane16_swap(fbits(x), fbits(x), false, false); return fmaxf(bitsf(s[0]), bitsf(s[1])); }
DI float max32(float x) { auto s = __builtin_amdgcn_permlane32_swap(fbits(x), fbits(x), false, false); return fmaxf(bitsf(s[0]), bitsf(s[1])); }
DI float wave_sum(float x) {
  x += dppf<0xB1>(x); x += dppf<0x4E>(x); x += dppf<0x141>(x); x += dppf<0x128>(x);
  return add32(add16(x));
}
DI float ex2(float x) { return __builtin_amdgcn_exp2f(x); }
constexpr float LOG2E = 1.4426950408889634f;

DI void lds_arrive(unsigned* c, int lane) {
  CBAR();
  if (lane == 0) __hip_atomic_fetch_add(c, 1u, __ATOMIC_RELAXED, __HIP_MEMORY_SCOPE_WORKGROUP);
  CBAR();
}
template <int SLEEP> DI void lds_wait(unsigned* c, unsigned target) {
#pragma unroll 1
  for (int i = 0; i < LCAP; ++i) {
    unsigned v = __hip_atomic_load(c, __ATOMIC_RELAXED, __HIP_MEMORY_SCOPE_WORKGROUP);
    if (__builtin_amdgcn_readfirstlane(v) >= target) break;
    if (SLEEP) __builtin_amdgcn_s_sleep(SLEEP);
    CBAR();
  }
  CBAR();
}
DI unsigned umax4(v4u x) { return max(max(x.x, x.y), max(x.z, x.w)); }
template <int N> DI void poll4(v4u (&v)[N], rsrc_t r, int voff, int sbase, int sstride) {
  CBAR();
#pragma unroll 1
  for (int i = 0; i < GCAP; ++i) {
#pragma unroll
    for (int n = 0; n < N; ++n) v[n] = ld_sc(r, voff, sbase + n * sstride);
    unsigned m = 0;
#pragma unroll
    for (int n = 0; n < N; ++n) m = max(m, umax4(v[n]));
    if (__builtin_amdgcn_ballot_w64(m == SENT) == 0) break;
    __builtin_amdgcn_s_sleep(1);
    CBAR();
  }
  CBAR();
}
DI void tr8(const v4u (&w)[8], v4u (&f)[8]) {
#pragma unroll
  for (int m = 0; m < 4; ++m)
#pragma unroll
    for (int p = 0; p < 4; ++p) {
      f[2 * m][p] = __builtin_amdgcn_perm(w[2 * p + 1][m], w[2 * p][m], 0x05040100u);
      f[2 * m + 1][p] = __builtin_amdgcn_perm(w[2 * p + 1][m], w[2 * p][m], 0x07060302u);
    }
}
DI void kfold_store(const f32x4 (&d)[8], float* slot, int lane) {
  const bool lo = (lane & 8) == 0;
  float o[8];
#pragma unroll
  for (int j = 0; j < 8; ++j) {
    float x = lo ? d[j][0] : d[j][3], y = lo ? d[j][2] : d[j][1];
    o[j] = x + dppf<0x128>(y);
  }
  *(f32x4*)(slot + lane * 8) = f32x4{o[0], o[1], o[2], o[3]};
  *(f32x4*)(slot + lane * 8 + 4) = f32x4{o[4], o[5], o[6], o[7]};
}
DI v4u canon4(f32x4 s) { return v4u{canon(fbits(s[0])), canon(fbits(s[1])), canon(fbits(s[2])), canon(fbits(s[3]))}; }
DI f32x4 asf(v4u x) { return __builtin_bit_cast(f32x4, x); }
DI v4u silu8(v4u c) {
  v4u r;
#pragma unroll
  for (int m = 0; m < 4; ++m) {
    float a = bflo(c[m]), b = bfhi(c[m]);
    a = a / (1.f + ex2(-a * LOG2E)); b = b / (1.f + ex2(-b * LOG2E));
    r[m] = pk2(a, b);
  }
  return r;
}
DI void ln_stats(const float (&x)[16], float& mean, float& den) {
  const float x0 = bitsf(__builtin_amdgcn_readfirstlane(fbits(x[0])));
  float s = 0.f, q = 0.f;
#pragma unroll
  for (int i = 0; i < 16; ++i) { float d = x[i] - x0; s += d; q = fmaf(d, d, q); }
  s = wave_sum(s); q = wave_sum(q);
  const float md = s * (1.f / 1024.f);
  float var = q * (1.f / 1024.f) - md * md;
  var = var < 0.f ? 0.f : var;
  mean = rbf(x0 + md);
  den = rbf(__builtin_sqrtf(rbf(rbf(var) + 1e-5f)));
}

struct Args {
  const u16 *x, *kc, *vc, *wq, *wo, *wg, *wu, *wd, *c, *wmod;
  u16* out;
  char* ws;
};

#define STAMP(i) do { } while (0)

__global__ void __launch_bounds__(768, 3) mega(Args a) {
  __shared__ __attribute__((aligned(16))) unsigned cnt[C_N];
  __shared__ __attribute__((aligned(16))) unsigned qs[8 * 68];
  __shared__ __attribute__((aligned(16))) float redC[8 * 512];
  __shared__ __attribute__((aligned(16))) float mlw[8 * 16];
  __shared__ __attribute__((aligned(16))) float mlf[16];
  __shared__ __attribute__((aligned(16))) float redO[8 * 1024];
  __shared__ __attribute__((aligned(16))) float redT[8 * 1024];
  __shared__ __attribute__((aligned(16))) float redU[8 * 512];

  const int bid = blockIdx.x, lane = threadIdx.x & 63;
  const int w = __builtin_amdgcn_readfirstlane(threadIdx.x >> 6);
  const int xc = bid & 7, jx = bid >> 3;
  const int ab = xc, akvh = jx >> 3, as = jx & 7;
  const int grp = ab * 4 + akvh;
  const bool typeB = jx >= 16;
  const int jb = jx - 16;
  const int g = lane >> 4, n = lane & 15;
  const int arow = (n >> 1) * 2048 + (n & 1) * 64 + g * 16;
  const rsrc_t rws = mk(a.ws, WS_BYTES);
  if (threadIdx.x < C_N) cnt[threadIdx.x] = 0;

  if (w < 8) {
    STAMP(0 + 0 * w);
    {
      const v4u S = {SENT, SENT, SENT, SENT};
      st_sc(S, rws, lane * 16, WS_ARENA + bid * ARENA_PER + w * 1024);
      const int m1slot = (bid & 31) * 8 + (bid >> 5);
      st_sc(S, rws, lane * 16, w < 3 ? WS_ARENA + bid * ARENA_PER + (8 + w) * 1024 : (w < 5 ? WS_MPA + m1slot * MPA_SLOT + (w - 3) * 1024 : WS_BYTES));
    }
    const rsrc_t rk = mk(a.kc, 64u << 20), rv = mk(a.vc, 64u << 20);
    const int kvbase = (grp * 8 + as) * (256 << 10) + w * (32 << 10);
    const int koff = kvbase + ((lane >> 2) & 3) * 256 + g * 64 + (lane & 3) * 16;
    const int voff = kvbase + lane * 16;
    const bool l0 = lane & 1, l1 = lane & 2;
    const int ta = xc * 14 + jx;
    const bool doT3 = jx < 14;
    const int hp = 2 * xc + (jb >> 3), cg = jb & 7;
    const int td = xc * 14 + jb;
    const bool doT4 = typeB && jb < 14;
    v4u k[32];
    v4u wt[16];
    v4u v[32];
    f32x4 S[8];
    v4u qf[4];
    auto ktr = [&](int t) {
#pragma unroll
      for (int m = 0; m < 4; ++m) {
        unsigned r0 = k[4 * t][m], r1 = k[4 * t + 1][m], r2 = k[4 * t + 2][m], r3 = k[4 * t + 3][m];
        const unsigned d0 = dppu<0xB1>(r0), d1 = dppu<0xB1>(r1), d2 = dppu<0xB1>(r2), d3 = dppu<0xB1>(r3);
        const unsigned s0 = l0 ? d1 : r0, s1 = l0 ? r1 : d0, s2 = l0 ? d3 : r2, s3 = l0 ? r3 : d2;
        const unsigned e0 = dppu<0x4E>(s0), e1 = dppu<0x4E>(s1), e2 = dppu<0x4E>(s2), e3 = dppu<0x4E>(s3);
        k[4 * t][m] = l1 ? e2 : s0; k[4 * t + 2][m] = l1 ? s2 : e0;
        k[4 * t + 1][m] = l1 ? e3 : s1; k[4 * t + 3][m] = l1 ? s3 : e1;
      }
      asm volatile("" : "+v"(k[4 * t]), "+v"(k[4 * t + 1]), "+v"(k[4 * t + 2]), "+v"(k[4 * t + 3]));
      SB0();
    };
    SB0();
#pragma unroll
    for (int i = 0; i < 32; ++i) {
      k[i] = ld_nt(rk, koff, i * 1024);
      SB0();
      if (i == KWIN - 1) {
        asm volatile("s_waitcnt lgkmcnt(0)\n\ts_barrier\n\ts_waitcnt vmcnt(%0)" :: "n"(KWIN) : "memory");
        lds_arrive(&cnt[C_RESET], lane);
      }
      if (i >= KWIN - 1 && i < 31) asm volatile("s_waitcnt vmcnt(%0)" :: "n"(KWIN - 1) : "memory");
      SB0();
      if (i >= KWIN + 2 && ((i - KWIN + 2) & 3) == 0 && i < 31) ktr((i - KWIN + 2) / 4 - 1);
    }
    constexpr int KDONE = (30 - KWIN + 2) / 4;
    if (w == 0) STAMP(1);
    lds_wait<2>(&cnt[C_QG], 4);
    if (w == 0) STAMP(2);
#pragma unroll
    for (int i = 0; i < 4; ++i) qf[i] = *(const v4u*)(qs + (n & 7) * 68 + g * 16 + i * 4);
#pragma unroll
    for (int u = 0; u < 8; ++u) {
      if (u >= KDONE) ktr(u);
      f32x4 s = {0.f, 0.f, 0.f, 0.f};
#pragma unroll
      for (int i = 0; i < 4; ++i) s = mfma(k[4 * u + i], qf[i], s);
      S[u] = s;
      SB0();
      if (u == 1 || u == 4) {
#pragma unroll
        for (int i = 0; i < 4; ++i) v[(u >> 2) * 4 + i] = ld_nt(rv, voff, ((u >> 2) * 4 + i) * 1024);
      }
      SB0();
    }
    float mw = -INFINITY;
#pragma unroll
    for (int t = 0; t < 8; ++t) {
      unsigned p0 = pk2(S[t][0], S[t][1]), p1 = pk2(S[t][2], S[t][3]);
      const float sc = 0.08838834764831845f;
      p0 = pk2(bflo(p0) * sc, bfhi(p0) * sc); p1 = pk2(bflo(p1) * sc, bfhi(p1) * sc);
      S[t] = f32x4{bflo(p0), bfhi(p0), bflo(p1), bfhi(p1)};
      mw = fmaxf(mw, fmaxf(fmaxf(S[t][0], S[t][1]), fmaxf(S[t][2], S[t][3])));
    }
    SB0();
#pragma unroll
    for (int i = 8; i < 12; ++i) v[i] = ld_nt(rv, voff, i * 1024);
    SB0();
    mw = max32(max16(mw));
    float lw = 0.f;
    const float nm = -mw * LOG2E;
    v4u P[4];
#pragma unroll
    for (int t = 0; t < 8; ++t) {
      float e0 = ex2(fmaf(S[t][0], LOG2E, nm)), e1 = ex2(fmaf(S[t][1], LOG2E, nm)), e2 = ex2(fmaf(S[t][2], LOG2E, nm)), e3 = ex2(fmaf(S[t][3], LOG2E, nm));
      lw += (e0 + e1) + (e2 + e3);
      P[t >> 1][(t & 1) * 2] = pk2(e0, e1); P[t >> 1][(t & 1) * 2 + 1] = pk2(e2, e3);
    }
    asm volatile("" : "+v"(P[0]), "+v"(P[1]), "+v"(P[2]), "+v"(P[3]));
    SB0();
#pragma unroll
    for (int i = 12; i < 16; ++i) v[i] = ld_nt(rv, voff, i * 1024);
    SB0();
    lw = add32(add16(lw));
    {
      if (lane < 8) *(f32x2*)(mlw + w * 16 + lane * 2) = f32x2{mw, lw};
      CBAR();
      unsigned old = 0;
      if (lane == 0) old = __hip_atomic_fetch_add(&cnt[C_MLW], 1u, __ATOMIC_RELAXED, __HIP_MEMORY_SCOPE_WORKGROUP);
      old = __builtin_amdgcn_readfirstlane(old);
      CBAR();
      if (old == 7) {
        f32x2 r = *(const f32x2*)(mlw + lane * 2);
        float M = max32(max16(max8(r[0])));
        float L = add32(add16(add8(r[1] * ex2((r[0] - M) * LOG2E))));
        if (lane < 8) {
          *(f32x2*)(mlf + lane * 2) = f32x2{M, 1.f / L};
          st_sc2(v2u{canon(fbits(M)), canon(fbits(L))}, rws, lane * 512, WS_ML + akvh * 4096 + ab * 64 + as * 8);
        }
        lds_arrive(&cnt[C_MLF], lane);
      }
    }
    if (w == 0) STAMP(3);
    f32x4 O[8];
#pragma unroll
    for (int j = 0; j < 8; ++j) O[j] = f32x4{0.f, 0.f, 0.f, 0.f};
    v4u sbcb[4];
    const int auxv = typeB ? (n >> 1) * 64 : arow;
    const int auxb = typeB ? WS_ML + (2 * hp + (w >> 2)) * 512 : WS_SB + w * 256;
    const int auxh = typeB ? 32 : 16384, auxl = typeB ? 16 : 128;
    const rsrc_t rw0 = mk(typeB ? a.wo : (ta >= 56 ? a.wu : a.wg), typeB ? (8u << 20) : (doT3 ? (7u << 20) : 0u));
    const int wstride = typeB ? 2048 : 7168;
    const int woff = typeB ? ((2 * hp + (w >> 2)) * 128 + (w & 3) * 32 + g * 8) * 2048 + cg * 256 + n * 16
                           : (w * 128 + (n >> 3) * 32 + g * 8) * 7168 + (ta % 56) * 128 + (n & 7) * 16;
#pragma unroll
    for (int blk = 0; blk < 4; ++blk) {
#pragma unroll
      for (int m = 0; m < 4; ++m) {
        v4u f0, f1;
#pragma unroll
        for (int p = 0; p < 4; ++p) {
          f0[p] = __builtin_amdgcn_perm(v[8 * blk + 2 * p + 1][m], v[8 * blk + 2 * p][m], 0x05040100u);
          f1[p] = __builtin_amdgcn_perm(v[8 * blk + 2 * p + 1][m], v[8 * blk + 2 * p][m], 0x07060302u);
        }
        O[2 * m] = mfma(f0, P[blk], O[2 * m]);
        O[2 * m + 1] = mfma(f1, P[blk], O[2 * m + 1]);
        SB0();
      }
      if (blk < 2) {
#pragma unroll
        for (int i = 0; i < 8; ++i) v[8 * blk + 16 + i] = ld_nt(rv, voff, (8 * blk + 16 + i) * 1024);
      } else if (blk == 2) {
#pragma unroll
        for (int i = 0; i < 8; ++i) wt[i] = ld_nt(rw0, woff, i * wstride);
      } else {
        if (doT3) {
#pragma unroll
          for (int i = 0; i < 8; ++i) wt[8 + i] = ld_nt(rw0, woff, (64 + i) * 7168);
        }
#pragma unroll
        for (int i = 0; i < 4; ++i) sbcb[i] = ld_sc(rws, auxv, auxb + (i >> 1) * auxh + (i & 1) * auxl);
      }
      SB0();
    }
    if (w == 0) STAMP(4);
    lds_wait<1>(&cnt[C_MLF], 1);
    {
      f32x2 ml = *(const f32x2*)(mlf + (n & 7) * 2);
      const float fs = ex2((mw - ml[0]) * LOG2E) * ml[1];
      if (n < 8) {
        float* dst = redO + w * 1024 + n * 128 + g * 32;
#pragma unroll
        for (int r = 0; r < 4; ++r) {
          *(f32x4*)(dst + r * 8) = f32x4{O[0][r] * fs, O[1][r] * fs, O[2][r] * fs, O[3][r] * fs};
          *(f32x4*)(dst + r * 8 + 4) = f32x4{O[4][r] * fs, O[5][r] * fs, O[6][r] * fs, O[7][r] * fs};
        }
      }
      lds_arrive(&cnt[C_O], lane);
      lds_wait<0>(&cnt[C_O], 8);
      const float* src = redO + (lane >> 5) * 4096 + w * 128 + (lane & 31) * 4;
      f32x4 s = (*(const f32x4*)src + *(const f32x4*)(src + 1024)) + (*(const f32x4*)(src + 2048) + *(const f32x4*)(src + 3072));
      s = f32x4{add32(s[0]), add32(s[1]), add32(s[2]), add32(s[3])};
      const int head = akvh * 8 + w;
      if (lane < 32)
        st_sc2(v2u{canon(pk2(s[0], s[1])), canon(pk2(s[2], s[3]))}, rws, (lane >> 3) * 4096 + (lane & 7) * 8,
               WS_OP + head * 16384 + (as >> 1) * 1024 + ab * 128 + (as & 1) * 64);
    }
    if (w == 0) STAMP(5);

    v4u f[8];
    tr8((const v4u(&)[8])wt[0], f);
    asm volatile("" : "+v"(f[0]), "+v"(f[1]), "+v"(f[2]), "+v"(f[3]), "+v"(f[4]), "+v"(f[5]), "+v"(f[6]), "+v"(f[7]));
    SB0();
    CBAR();
#pragma unroll 1
    for (int i = 0; i < GCAP; ++i) {
      unsigned m = max(max(umax4(sbcb[0]), umax4(sbcb[1])), max(umax4(sbcb[2]), umax4(sbcb[3])));
      if (__builtin_amdgcn_ballot_w64(m == SENT) == 0) break;
      __builtin_amdgcn_s_sleep(1);
      CBAR();
#pragma unroll
      for (int j = 0; j < 4; ++j) sbcb[j] = ld_sc(rws, auxv, auxb + (j >> 1) * auxh + (j & 1) * auxl);
    }
    CBAR();
    float F[4];
    {
      float M = -INFINITY, tot = 0.f;
#pragma unroll
      for (int p = 0; p < 4; ++p) M = fmaxf(M, fmaxf(bitsf(sbcb[p][0]), bitsf(sbcb[p][2])));
#pragma unroll
      for (int p = 0; p < 4; ++p) {
        const float w0 = bitsf(sbcb[p][1]) * ex2((bitsf(sbcb[p][0]) - M) * LOG2E), w1 = bitsf(sbcb[p][3]) * ex2((bitsf(sbcb[p][2]) - M) * LOG2E);
        tot += w0 + w1;
        F[p] = l0 ? w1 : w0;
      }
      tot = 1.f / tot;
#pragma unroll
      for (int p = 0; p < 4; ++p) F[p] *= tot;
    }
    asm volatile("" : "+v"(F[0]), "+v"(F[1]), "+v"(F[2]), "+v"(F[3]));
    if (typeB) {
      v4u oa[4];
      poll4<4>(oa, rws, n * 64 + g * 16, WS_OP + (2 * hp + (w >> 2)) * 16384 + (w & 3) * 4096, 1024);
      if (w == 0) STAMP(6);
      v4u oo;
#pragma unroll
      for (int m = 0; m < 4; ++m) {
        float lo = bflo(oa[0][m]) * F[0], hi = bfhi(oa[0][m]) * F[0];
#pragma unroll
        for (int p = 1; p < 4; ++p) { lo = fmaf(bflo(oa[p][m]), F[p], lo); hi = fmaf(bfhi(oa[p][m]), F[p], hi); }
        oo[m] = pk2(lo + dppf<0xB1>(lo), hi + dppf<0xB1>(hi));
      }
      f32x4 d[8];
#pragma unroll
      for (int j = 0; j < 8; ++j) d[j] = mfma(oo, f[j], f32x4{0.f, 0.f, 0.f, 0.f});
      float* dst = redT + w * 1024 + g * 256 + n * 8;
      *(f32x4*)(dst) = f32x4{d[0][0], d[1][0], d[2][0], d[3][0]};
      *(f32x4*)(dst + 4) = f32x4{d[4][0], d[5][0], d[6][0], d[7][0]};
      *(f32x4*)(dst + 128) = f32x4{d[0][2], d[1][2], d[2][2], d[3][2]};
      *(f32x4*)(dst + 132) = f32x4{d[4][2], d[5][2], d[6][2], d[7][2]};
      lds_arrive(&cnt[C_T1], lane);
      lds_wait<0>(&cnt[C_T1], 8);
      const float* src = redT + (lane >> 5) * 4096 + w * 128 + (lane & 31) * 4;
      f32x4 s = (*(const f32x4*)src + *(const f32x4*)(src + 1024)) + (*(const f32x4*)(src + 2048) + *(const f32x4*)(src + 3072));
      s = f32x4{add32(s[0]), add32(s[1]), add32(s[2]), add32(s[3])};
      if (lane < 32) st_sc(canon4(s), rws, (lane >> 3) * 2048 + (lane & 7) * 16, WS_XP + (w * 32 + cg * 4) * 2048 + hp * 128);
      if (w == 0) STAMP(7);
      if (doT4) {
        const int ks = td >> 4, c16 = td & 15;
        const rsrc_t rw = mk(a.wd, 7u << 20);
        const int off = (ks * 512 + w * 64 + (n >> 3) * 32 + g * 8) * 2048 + c16 * 128 + (n & 7) * 16;
#pragma unroll
        for (int i = 0; i < 8; ++i) wt[i] = ld_nt(rw, off, i * 2048);
        tr8((const v4u(&)[8])wt[0], f);
        asm volatile("" : "+v"(f[0]), "+v"(f[1]), "+v"(f[2]), "+v"(f[3]), "+v"(f[4]), "+v"(f[5]), "+v"(f[6]), "+v"(f[7]));
        if (w == 0) STAMP(12);
        v4u gu[2];
        poll4<2>(gu, rws, n * 64 + g * 16, WS_GU + (ks * 8 + w) * 1024, 56 * 1024);
        if (w == 0) STAMP(13);
        v4u hh;
#pragma unroll
        for (int m = 0; m < 4; ++m) {
          float g0 = bflo(gu[0][m]), g1 = bfhi(gu[0][m]);
          g0 = g0 > 0.f ? g0 : g0 * 0.f; g1 = g1 > 0.f ? g1 : g1 * 0.f;
          hh[m] = pk2(g0 * bflo(gu[1][m]), g1 * bfhi(gu[1][m]));
        }
#pragma unroll
        for (int j = 0; j < 8; ++j) d[j] = mfma(hh, f[j], f32x4{0.f, 0.f, 0.f, 0.f});
        kfold_store(d, redU + w * 512, lane);
        lds_arrive(&cnt[C_T4], lane);
        lds_wait<0>(&cnt[C_T4], 8);
        const float* s2 = redU + g * 1024 + w * 64 + n * 4;
        f32x4 t = *(const f32x4*)s2 + *(const f32x4*)(s2 + 512);
        t = f32x4{add32(add16(t[0])), add32(add16(t[1])), add32(add16(t[2])), add32(add16(t[3]))};
        if (lane < 16) st_sc(canon4(t), rws, (lane >> 3) * 1024 + (lane & 7) * 16, WS_DP + (w * 32 + c16 * 2) * 1024 + ks * 128);
        if (w == 0) STAMP(14);
      }
    } else if (doT3) {
      v4u f1[8];
      tr8((const v4u(&)[8])wt[8], f1);
      asm volatile("" : "+v"(f1[0]), "+v"(f1[1]), "+v"(f1[2]), "+v"(f1[3]), "+v"(f1[4]), "+v"(f1[5]), "+v"(f1[6]), "+v"(f1[7]));
      SB0();
      float cb1[16], sbf[16];
#pragma unroll
      for (int i = 0; i < 8; ++i) {
        unsigned u = sbcb[2 + (i >> 2)][i & 3];
        unsigned r = pk2(1.f + bflo(u), 1.f + bfhi(u));
        cb1[2 * i] = bflo(r); cb1[2 * i + 1] = bfhi(r);
        u = sbcb[i >> 2][i & 3];
        sbf[2 * i] = bflo(u); sbf[2 * i + 1] = bfhi(u);
        asm volatile("" : "+v"(cb1[2 * i]), "+v"(cb1[2 * i + 1]), "+v"(sbf[2 * i]), "+v"(sbf[2 * i + 1]));
      }
      if (w == 0) STAMP(9);
      v4u xr[4];
      {
        const int sb0 = WS_X1 + xc * REPX_STRIDE, vst = 16384 + (n >> 1) * 256 + (g * 2 + (n & 1)) * 32;
        CBAR();
#pragma unroll 1
        for (int i = 0; i < GCAP; ++i) {
          xr[2] = ld_sc(rws, vst, sb0); xr[3] = ld_sc(rws, vst, sb0 + 16);
          xr[0] = ld_sc(rws, arow, sb0 + w * 256); xr[1] = ld_sc(rws, arow, sb0 + w * 256 + 128);
          unsigned m = max(max(umax4(xr[0]), umax4(xr[1])), max(umax4(xr[2]), umax4(xr[3])));
          if (__builtin_amdgcn_ballot_w64(m == SENT) == 0) break;
          __builtin_amdgcn_s_sleep(1);
          CBAR();
        }
        CBAR();
      }
      if (w == 0) STAMP(10);
      float mean, rden;
      {
        const float m0 = bitsf(xr[2][0]), m1 = bitsf(xr[2][2]), m2 = bitsf(xr[3][0]), m3 = bitsf(xr[3][2]);
        float s = (m0 + m1) + (m2 + m3);
        s += dppf<0xB1>(s);
        const float mu = add32(add16(s)) * (1.f / 32.f);
        float q = fmaf(m0 - mu, m0 - mu, (m1 - mu) * (m1 - mu)) + fmaf(m2 - mu, m2 - mu, (m3 - mu) * (m3 - mu));
        q = fmaf(q, 32.f, (bitsf(xr[2][1]) + bitsf(xr[2][3])) + (bitsf(xr[3][1]) + bitsf(xr[3][3])));
        q += dppf<0xB1>(q);
        const float var = add32(add16(q)) * (1.f / 1024.f);
        mean = rbf(mu);
        rden = 1.f / rbf(__builtin_sqrtf(rbf(rbf(var) + 1e-5f)));
      }
      v4u ho[2];
#pragma unroll
      for (int i = 0; i < 8; ++i) {
        unsigned u = xr[i >> 2][i & 3];
        u = pk2(bflo(u) - mean, bfhi(u) - mean);
        u = pk2(bflo(u) * rden, bfhi(u) * rden);
        u = pk2(bflo(u) * cb1[2 * i], bfhi(u) * cb1[2 * i + 1]);
        ho[i >> 2][i & 3] = pk2(bflo(u) + sbf[2 * i], bfhi(u) + sbf[2 * i + 1]);
      }
      const v4u a0 = ho[0], a1 = ho[1];
      f32x4 d[8];
#pragma unroll
      for (int j = 0; j < 8; ++j) d[j] = mfma(a1, f1[j], mfma(a0, f[j], f32x4{0.f, 0.f, 0.f, 0.f}));
      kfold_store(d, redT + w * 512, lane);
      lds_arrive(&cnt[C_T3], lane);
      lds_wait<0>(&cnt[C_T3], 8);
      const float* s2 = redT + g * 1024 + w * 64 + n * 4;
      f32x4 t = *(const f32x4*)s2 + *(const f32x4*)(s2 + 512);
      t = f32x4{add32(add16(t[0])), add32(add16(t[1])), add32(add16(t[2])), add32(add16(t[3]))};
      if (lane < 16) st_sc2(v2u{canon(pk2(t[0], t[1])), canon(pk2(t[2], t[3]))}, rws, lane * 8, WS_GU + ta * 1024 + w * 128);
      if (w == 0) STAMP(11);
    }
  } else {
    const int cw = w - 8;
    __builtin_amdgcn_s_setprio(2);
    const rsrc_t rc = mk(a.c, 16384), rm = mk(a.wmod, 12u << 20);
    const int wlane_r = (n >> 3) * 32 + g * 8, wlane_c = (n & 7) * 16;
    const int rb = xc, rcb = 2 * jb + cw, rr = rb * 32 + rcb, piece = lane & 7;
    float xo[4] = {0.f, 0.f, 0.f, 0.f};
    if (cw >= 2) {
      const int h = cw - 2;
      const int ct = bid & 31, ks = bid >> 5;
      const int qct = bid & 63, qks = bid >> 6;
      v4u wm[8], wq0[8], wq1[8];
      const v4u cv = ld_pl(rc, arow, (ks * 128 + h * 64) * 2);
      {
        const int off = (ks * 128 + h * 64 + wlane_r) * 12288 + ct * 128 + wlane_c;
#pragma unroll
        for (int i = 0; i < 8; ++i) wm[i] = ld_nt(rm, off, i * 12288);
      }
      SB0();
      asm volatile("s_waitcnt lgkmcnt(0)\n\ts_barrier" ::: "memory");
      const v4u ca = silu8(cv);
      v4u f[8];
      f32x4 d[8];
      tr8(wm, f);
      SB0();
      {
        const rsrc_t rq = mk(a.wq, 8u << 20);
        const int off = (qks * 256 + h * 128 + wlane_r) * 8192 + qct * 128 + wlane_c;
#pragma unroll
        for (int i = 0; i < 8; ++i) wq0[i] = ld_nt(rq, off, i * 8192);
#pragma unroll
        for (int i = 0; i < 8; ++i) wq1[i] = ld_nt(rq, off, (64 + i) * 8192);
      }
      SB0();
#pragma unroll
      for (int j = 0; j < 8; ++j) d[j] = mfma(ca, f[j], f32x4{0.f, 0.f, 0.f, 0.f});
      kfold_store(d, redC + h * 512, lane);
      lds_arrive(&cnt[C_M1], lane);
      lds_wait<0>(&cnt[C_M1], 2);
      {
        f32x4 s = *(const f32x4*)(redC + h * 256 + lane * 4) + *(const f32x4*)(redC + 512 + h * 256 + lane * 4);
        lds_wait<1>(&cnt[C_RESET], 8);
        st_sc(canon4(s), rws, lane * 16, WS_MPA + (ct * 8 + ks) * MPA_SLOT + h * 1024);
      }
      if (cw == 2) STAMP(16);
      v4u f2[8];
      tr8(wq0, f);
      tr8(wq1, f2);
      asm volatile("" : "+v"(f[0]), "+v"(f[1]), "+v"(f[2]), "+v"(f[3]), "+v"(f[4]), "+v"(f[5]), "+v"(f[6]), "+v"(f[7]));
      asm volatile("" : "+v"(f2[0]), "+v"(f2[1]), "+v"(f2[2]), "+v"(f2[3]), "+v"(f2[4]), "+v"(f2[5]), "+v"(f2[6]), "+v"(f2[7]));
      lds_wait<1>(&cnt[C_GO], 1);
      v4u ha[2];
      poll4<2>(ha, rws, arow, WS_H + xc * REP_STRIDE + (qks * 256 + h * 128) * 2, 128);
      if (cw == 2) STAMP(19);
#pragma unroll
      for (int j = 0; j < 8; ++j) d[j] = mfma(ha[1], f2[j], mfma(ha[0], f[j], f32x4{0.f, 0.f, 0.f, 0.f}));
      kfold_store(d, redC + 1024 + h * 512, lane);
      lds_arrive(&cnt[C_Q], lane);
      lds_wait<0>(&cnt[C_Q], 2);
      {
        f32x4 s = *(const f32x4*)(redC + 1024 + h * 256 + lane * 4) + *(const f32x4*)(redC + 1536 + h * 256 + lane * 4);
        st_sc(canon4(s), rws, g * 65536 + n * 16, WS_QP + h * 4 * 65536 + (qct >> 4) * 16384 + qks * 4096 + (qct & 15) * 256);
      }
      if (cw == 2) STAMP(20);
    } else {
      u64 ep = 0;
      v4u xr[2];
      v2u xo2 = {0u, 0u};
      if (cw == 0) { v2u e = ld_sc2(rws, 0, WS_EPOCH); ep = ((u64)e.y << 32) | e.x; }
      if (typeB) {
        const rsrc_t rx = mk(a.x, 16384);
        xr[0] = ld_pl(rx, lane * 16, rb * 2048); xr[1] = ld_pl(rx, lane * 16, rb * 2048 + 1024);
        xo2 = ld_pl2(rx, piece * 8, rb * 2048 + rcb * 64);
      }
      asm volatile("s_waitcnt lgkmcnt(0)\n\ts_barrier" ::: "memory");
      if (cw == 0) {
        const u64 tag = ep + 0x9E3779B97F4A7C15ull;
        const unsigned tlo = (unsigned)tag, thi = (unsigned)(tag >> 32);
        lds_wait<1>(&cnt[C_RESET], 8);
        if (lane < 8) st_sc2(v2u{tlo, thi}, rws, lane * FLAG_REP, WS_FLAGS + bid * 8);
        STAMP(15);
        CBAR();
#pragma unroll 1
        for (int i = 0; i < GCAP; ++i) {
          v4u f0 = ld_sc(rws, lane * 16, WS_FLAGS + xc * FLAG_REP), f1 = ld_sc(rws, lane * 16, WS_FLAGS + xc * FLAG_REP + 1024);
          bool bad = (f0.x != tlo) | (f0.y != thi) | (f0.z != tlo) | (f0.w != thi) | (f1.x != tlo) | (f1.y != thi) | (f1.z != tlo) | (f1.w != thi);
          if (__builtin_amdgcn_ballot_w64(bad) == 0) break;
          __builtin_amdgcn_s_sleep(1);
          CBAR();
        }
        lds_arrive(&cnt[C_GO], lane);
        if (bid == 0 && lane == 0) st_sc2(v2u{tlo, thi}, rws, 0, WS_EPOCH);
        STAMP(17);
      }
      if (typeB) {
        float xv[16];
#pragma unroll
        for (int i = 0; i < 8; ++i) { xv[2 * i] = bflo(xr[i >> 2][i & 3]); xv[2 * i + 1] = bfhi(xr[i >> 2][i & 3]); }
        float mean, den;
        ln_stats(xv, mean, den);
        xo[0] = bflo(xo2.x); xo[1] = bfhi(xo2.x); xo[2] = bflo(xo2.y); xo[3] = bfhi(xo2.y);
        float ln[4];
#pragma unroll
        for (int i = 0; i < 4; ++i) ln[i] = rbf(rbf(xo[i] - mean) / den);
        lds_wait<1>(&cnt[C_GO], 1);
        v4u mp[2];
        poll4<2>(mp, rws, (lane >> 3) * MPA_SLOT + piece * 16, WS_MPA + (rcb >> 1) * 8 * MPA_SLOT + rb * 256 + (rcb & 1) * 128, 128 * MPA_SLOT);
        float hv[4];
#pragma unroll
        for (int i = 0; i < 4; ++i) {
          float sa = rbf(add32(add16(add8(bitsf(mp[0][i]))))), ca = rbf(add32(add16(add8(bitsf(mp[1][i])))));
          hv[i] = rbf(ln[i] * rbf(1.f + ca)) + sa;
        }
        st_sc2(v2u{canon(pk2(hv[0], hv[1])), canon(pk2(hv[2], hv[3]))}, rws, (lane >> 3) * REP_STRIDE + piece * 8, WS_H + rb * 2048 + rcb * 64);
        if (cw == 0) STAMP(18);
      } else {
        lds_wait<1>(&cnt[C_GO], 1);
      }
    }
    {
      v4u qp[4];
      poll4<4>(qp, rws, lane * 16, WS_QP + grp * 16384 + cw * 1024, 4096);
      f32x4 s = (asf(qp[0]) + asf(qp[1])) + (asf(qp[2]) + asf(qp[3]));
      *(v2u*)(qs + (cw * 2 + (lane >> 5)) * 68 + (lane & 31) * 2) = v2u{pk2(s[0], s[1]), pk2(s[2], s[3])};
      lds_arrive(&cnt[C_QG], lane);
      if (cw == 0) STAMP(21);
    }
    {
      const int ct = bid & 63, ks = bid >> 6;
      v4u w0[8];
      const int r0 = ks * 256 + cw * 64;
      const v4u c0 = ld_pl(rc, arow, r0 * 2);
      const int off = (r0 + wlane_r) * 12288 + 4096 + ct * 128 + wlane_c;
#pragma unroll
      for (int i = 0; i < 8; ++i) w0[i] = ld_nt(rm, off, i * 12288);
      SB0();
      const v4u a0 = silu8(c0);
      v4u f[8];
      tr8(w0, f);
      f32x4 d[8];
#pragma unroll
      for (int j = 0; j < 8; ++j) d[j] = mfma(a0, f[j], f32x4{0.f, 0.f, 0.f, 0.f});
      kfold_store(d, redC + 2048 + cw * 512, lane);
      lds_arrive(&cnt[C_M2], lane);
      lds_wait<1>(&cnt[C_M2], 4);
      const float* src = redC + 2048 + (lane >> 5) * 1024 + cw * 128 + (lane & 31) * 4;
      f32x4 s = *(const f32x4*)src + *(const f32x4*)(src + 512);
      s = f32x4{add32(s[0]), add32(s[1]), add32(s[2]), add32(s[3])};
      if (lane < 32) st_sc(canon4(s), rws, g * 65536 + (n >> 3) * 2048 + (n & 7) * 16, WS_MPB + cw * 131072 + (ct & 15) * 4096 + (ct >> 4) * 512 + ks * 128);
      if (cw == 0) STAMP(22);
    }
    if (typeB && cw < 2) {
      float ga[4], gb[4];
      {
        v4u mp[2];
        poll4<2>(mp, rws, lane * 16, WS_MPB + rr * 2048, 1024);
        float mv[8];
#pragma unroll
        for (int i = 0; i < 8; ++i) mv[i] = add16(add8(bitsf(mp[i >> 2][i & 3])));
        const unsigned u0 = pk2(mv[0], mv[1]), u1 = pk2(mv[2], mv[3]), u2 = pk2(mv[4], mv[5]), u3 = pk2(mv[6], mv[7]);
        if ((lane & 31) < 8) st_sc2(lane < 32 ? v2u{canon(u2), canon(u3)} : v2u{canon(u0), canon(u1)}, rws, (lane < 32 ? 16384 : 0) + piece * 8, WS_SB + rb * 2048 + rcb * 64);
        const unsigned a0 = __shfl(u0, piece), a1 = __shfl(u1, piece), b0 = __shfl(u2, 32 + piece), b1 = __shfl(u3, 32 + piece);
        ga[0] = bflo(a0); ga[1] = bfhi(a0); ga[2] = bflo(a1); ga[3] = bfhi(a1);
        gb[0] = bflo(b0); gb[1] = bfhi(b0); gb[2] = bflo(b1); gb[3] = bfhi(b1);
        if (cw == 0) STAMP(24);
      }
      lds_wait<4>(&cnt[C_O], 8);
      float x1[4];
      {
        v4u xp[2];
        poll4<2>(xp, rws, lane * 16, WS_XP + rr * 2048, 1024);
#pragma unroll
        for (int i = 0; i < 4; ++i) {
          float s = add32(add16(add8(bitsf(xp[0][i]) + bitsf(xp[1][i]))));
          x1[i] = xo[i] + rbf(ga[i] * rbf(s));
        }
        const unsigned u0 = pk2(x1[0], x1[1]), u1 = pk2(x1[2], x1[3]);
        st_sc2(v2u{canon(u0), canon(u1)}, rws, (lane >> 3) * REPX_STRIDE + piece * 8, WS_X1 + rb * 2048 + rcb * 64);
        x1[0] = bflo(u0); x1[1] = bfhi(u0); x1[2] = bflo(u1); x1[3] = bfhi(u1);
        const float x0 = bitsf(__builtin_amdgcn_readfirstlane(fbits(x1[0])));
        const float e0 = x1[0] - x0, e1 = x1[1] - x0, e2 = x1[2] - x0, e3 = x1[3] - x0;
        float sm = (e0 + e1) + (e2 + e3), sq = fmaf(e0, e0, e1 * e1) + fmaf(e2, e2, e3 * e3);
        sm += dppf<0xB1>(sm); sq += dppf<0xB1>(sq);
        sm += dppf<0x4E>(sm); sq += dppf<0x4E>(sq);
        sm += dppf<0x141>(sm); sq += dppf<0x141>(sq);
        const float md = sm * (1.f / 32.f);
        if (piece == 0) st_sc2(v2u{canon(fbits(x0 + md)), canon(fbits(fmaxf(sq - sm * md, 0.f)))}, rws, (lane >> 3) * REPX_STRIDE, WS_X1 + 16384 + rb * 256 + rcb * 8);
        lds_arrive(&cnt[C_T2], lane);
        if (cw == 0) STAMP(8);
      }
      {
        v4u dp[1];
        poll4<1>(dp, rws, (lane < 56 ? lane : lane - 8) * 16, WS_DP + rr * 1024, 0);
        float o[4];
#pragma unroll
        for (int i = 0; i < 4; ++i) {
          float s = add32(add16(add8(lane < 56 ? bitsf(dp[0][i]) : 0.f)));
          o[i] = x1[i] + rbf(gb[i] * rbf(s));
        }
        if (lane < 8) *(v2u*)(a.out + rb * 1024 + rcb * 32 + piece * 4) = v2u{pk2(o[0], o[1]), pk2(o[2], o[3])};
        if (cw == 0) STAMP(25);
      }
    }
  }
}

extern "C" void kernel_launch(void* const* d_in, const int* in_sizes, int n_in, void* d_out, int out_size, void* d_ws, size_t ws_size, hipStream_t stream) {
  Args a;
  a.x = (const u16*)d_in[0]; a.kc = (const u16*)d_in[1]; a.vc = (const u16*)d_in[2]; a.wq = (const u16*)d_in[3]; a.wo = (const u16*)d_in[4];
  a.wg = (const u16*)d_in[6]; a.wu = (const u16*)d_in[7]; a.wd = (const u16*)d_in[8]; a.c = (const u16*)d_in[9]; a.wmod = (const u16*)d_in[10];
  a.out = (u16*)d_out; a.ws = (char*)d_ws;
  mega<<<256, 768, 0, stream>>>(a);
}
```

```cpp
#include <hip/hip_runtime.h>
#include <stdint.h>

typedef unsigned short u16;
typedef unsigned long long u64;
typedef unsigned v4u __attribute__((ext_vector_type(4)));
typedef unsigned v2u __attribute__((ext_vector_type(2)));
typedef float f32x4 __attribute__((ext_vector_type(4)));
typedef float f32x2 __attribute__((ext_vector_type(2)));
typedef __bf16 bf16x8 __attribute__((ext_vector_type(8)));
typedef __bf16 bf16x2 __attribute__((ext_vector_type(2)));
typedef __amdgpu_buffer_rsrc_t rsrc_t;
#define DI __device__ __forceinline__
#define SB0() __builtin_amdgcn_sched_barrier(0)
#define CBAR() asm volatile("" ::: "memory")
#define PIN8(f) asm volatile("" : "+v"(f[0]), "+v"(f[1]), "+v"(f[2]), "+v"(f[3]), "+v"(f[4]), "+v"(f[5]), "+v"(f[6]), "+v"(f[7]))
#define VMWAIT(n) asm volatile("s_waitcnt vmcnt(%0)" :: "n"(n) : "memory")

#define STAMPS 0
#define KWIN 3
#define VWIN 4
#define WEARLY 4

constexpr int WS_EPOCH = 0;
constexpr int WS_FLAGS = 1024, FLAG_REP = 2304;
constexpr int WS_MPA = 32768, MPA_SLOT = 4096 + 256;
constexpr int WS_H = WS_MPA + 256 * MPA_SLOT, HREP = 32768 + 256;
constexpr int WS_ARENA = 2 << 20;
static_assert(WS_H + 8 * HREP <= WS_ARENA, "map");
constexpr int WS_QP = WS_ARENA;
constexpr int WS_OP = WS_QP + (512 << 10);
constexpr int WS_XP = WS_OP + (512 << 10);
constexpr int WS_MPB = WS_XP + (512 << 10);
constexpr int WS_DP = WS_MPB + (512 << 10);
constexpr int REPX = 16384 + 2048 + 256;
constexpr int WS_X1 = WS_DP + (256 << 10);
constexpr int WS_GU = WS_X1 + 8 * REPX;
constexpr int WS_SB = WS_GU + (112 << 10);
constexpr int WS_ML = WS_SB + (32 << 10);
constexpr int WS_END = WS_ML + (16 << 10);
constexpr int ARENA_PER = 11 * 1024;
static_assert(WS_END <= WS_ARENA + 256 * ARENA_PER, "arena");
constexpr int WS_BYTES = 8 << 20;
constexpr unsigned SENT = 0xFFFFFFFFu;
#define CAPSH 0
constexpr int GCAP = 1 << (15 - CAPSH);
constexpr int LCAP = 1 << (22 - CAPSH);

enum { C_RESET = 0, C_GO, C_M1, C_M1S, C_R1, C_Q, C_QS, C_QG, C_MLW, C_MLF, C_M2, C_VI, C_O, C_T1, C_T2, C_PRE, C_C12, C_T3, C_T4, C_N = 32 };

DI float bflo(unsigned v) { return __builtin_bit_cast(float, v << 16); }
DI float bfhi(unsigned v) { return __builtin_bit_cast(float, v & 0xffff0000u); }
DI unsigned pk2(float a, float b) { bf16x2 v = {(__bf16)a, (__bf16)b}; unsigned r = __builtin_bit_cast(unsigned, v); asm("" : "+v"(r)); return r; }
DI float rbf(float a) { return (float)(__bf16)a; }
DI unsigned canon(unsigned u) { return u < 0xFFFFFFFEu ? u : 0xFFFFFFFEu; }
DI unsigned fbits(float f) { return __builtin_bit_cast(unsigned, f); }
DI float bitsf(unsigned u) { return __builtin_bit_cast(float, u); }
DI rsrc_t mk(const void* p, unsigned bytes) { return __builtin_amdgcn_make_buffer_rsrc((void*)p, 0, bytes, 0x00020000); }
DI v4u ld_nt(rsrc_t r, int v, int s) { return __builtin_amdgcn_raw_buffer_load_b128(r, v, s, 2); }
DI v4u ld_pl(rsrc_t r, int v, int s) { return __builtin_amdgcn_raw_buffer_load_b128(r, v, s, 0); }
DI unsigned ld_pl1(rsrc_t r, int v, int s) { return __builtin_amdgcn_raw_buffer_load_b32(r, v, s, 0); }
DI v4u ld_sc(rsrc_t r, int v, int s) { return __builtin_amdgcn_raw_buffer_load_b128(r, v, s, 16); }
DI v2u ld_sc2(rsrc_t r, int v, int s) { return __builtin_amdgcn_raw_buffer_load_b64(r, v, s, 16); }
DI unsigned ld_sc1(rsrc_t r, int v, int s) { return __builtin_amdgcn_raw_buffer_load_b32(r, v, s, 16); }
DI void st_sc(v4u x, rsrc_t r, int v, int s) { __builtin_amdgcn_raw_buffer_store_b128(x, r, v, s, 16); }
DI void st_sc2(v2u x, rsrc_t r, int v, int s) { __builtin_amdgcn_raw_buffer_store_b64(x, r, v, s, 16); }
DI void st_sc1(unsigned x, rsrc_t r, int v, int s) { __builtin_amdgcn_raw_buffer_store_b32(x, r, v, s, 16); }
DI f32x4 mfma(v4u a, v4u b, f32x4 c) {
  return __builtin_amdgcn_mfma_f32_16x16x32_bf16(__builtin_bit_cast(bf16x8, a), __builtin_bit_cast(bf16x8, b), c, 0, 0, 0);
}
template <int CTRL> DI unsigned dppu(unsigned x) { return __builtin_amdgcn_update_dpp(0u, x, CTRL, 0xF, 0xF, true); }
template <int CTRL> DI float dppf(float x) { return bitsf(dppu<CTRL>(fbits(x))); }
DI float add8(float x) { return x + dppf<0x128>(x); }
DI float add16(float x) { auto s = __builtin_amdgcn_permlane16_swap(fbits(x), fbits(x), false, false); return bitsf(s[0]) + bitsf(s[1]); }
DI float add32(float x) { auto s = __builtin_amdgcn_permlane32_swap(fbits(x), fbits(x), false, false); return bitsf(s[0]) + bitsf(s[1]); }
DI float max8(float x) { return fmaxf(x, dppf<0x128>(x)); }
DI float max16(float x) { auto s = __builtin_amdgcn_permlane16_swap(fbits(x), fbits(x), false, false); return fmaxf(bitsf(s[0]), bitsf(s[1])); }
DI float max32(float x) { auto s = __builtin_amdgcn_permlane32_swap(fbits(x), fbits(x), false, false); return fmaxf(bitsf(s[0]), bitsf(s[1])); }
DI float row_sum(float x) { x += dppf<0xB1>(x); x += dppf<0x4E>(x); x += dppf<0x141>(x); return x + dppf<0x128>(x); }
DI float sum32(float x) { return add16(row_sum(x)); }
DI float wave_sum(float x) { return add32(sum32(x)); }
DI float ex2(float x) { return __builtin_amdgcn_exp2f(x); }
DI float rcp(float x) { return __builtin_amdgcn_rcpf(x); }
constexpr float LOG2E = 1.4426950408889634f;
DI unsigned pair_pack(float v) { return dppu<0xA0>(pk2(v, dppf<0xB1>(v))); }

DI void lds_arrive(unsigned* c, int lane) {
  CBAR();
  if (lane == 0) __hip_atomic_fetch_add(c, 1u, __ATOMIC_RELAXED, __HIP_MEMORY_SCOPE_WORKGROUP);
  CBAR();
}
template <int SLEEP> DI void lds_wait(unsigned* c, unsigned target) {
#pragma unroll 1
  for (int i = 0; i < LCAP; ++i) {
    unsigned v = __hip_atomic_load(c, __ATOMIC_RELAXED, __HIP_MEMORY_SCOPE_WORKGROUP);
    if (__builtin_amdgcn_readfirstlane(v) >= target) break;
    if (SLEEP) __builtin_amdgcn_s_sleep(SLEEP);
    CBAR();
  }
  CBAR();
}
DI unsigned umax4(v4u x) { return max(max(x.x, x.y), max(x.z, x.w)); }
template <int N, int SLEEP = 1> DI void poll4(v4u (&v)[N], rsrc_t r, int voff, int sbase, int sstride) {
  CBAR();
#pragma unroll 1
  for (int i = 0; i < GCAP; ++i) {
#pragma unroll
    for (int n = 0; n < N; ++n) v[n] = ld_sc(r, voff, sbase + n * sstride);
    unsigned m = 0;
#pragma unroll
    for (int n = 0; n < N; ++n) m = max(m, umax4(v[n]));
    if (__builtin_amdgcn_ballot_w64(m == SENT) == 0) break;
    __builtin_amdgcn_s_sleep(SLEEP);
    CBAR();
  }
  CBAR();
}
template <int N, int SLEEP = 1> DI void poll1(unsigned (&v)[N], rsrc_t r, int voff, int sbase, int sstride) {
  CBAR();
#pragma unroll 1
  for (int i = 0; i < GCAP; ++i) {
#pragma unroll
    for (int n = 0; n < N; ++n) v[n] = ld_sc1(r, voff, sbase + n * sstride);
    unsigned m = 0;
#pragma unroll
    for (int n = 0; n < N; ++n) m = max(m, v[n]);
    if (__builtin_amdgcn_ballot_w64(m == SENT) == 0) break;
    __builtin_amdgcn_s_sleep(SLEEP);
    CBAR();
  }
  CBAR();
}
DI void pollt(v4u (&v)[4], rsrc_t r, int voff, int sbase, int sh, int sl, unsigned ta, unsigned tb) {
  CBAR();
#pragma unroll 1
  for (int i = 0; i < GCAP; ++i) {
#pragma unroll
    for (int n = 0; n < 4; ++n) v[n] = ld_sc(r, voff, sbase + (n >> 1) * sh + (n & 1) * sl);
    unsigned m = 0;
#pragma unroll
    for (int n = 0; n < 4; ++n) m |= (v[n].y ^ ta) | (v[n].w ^ tb);
    if (__builtin_amdgcn_ballot_w64(m != 0) == 0) break;
    __builtin_amdgcn_s_sleep(1);
    CBAR();
  }
  CBAR();
}
DI void tr8(const v4u (&w)[8], v4u (&f)[8]) {
#pragma unroll
  for (int m = 0; m < 4; ++m)
#pragma unroll
    for (int p = 0; p < 4; ++p) {
      f[2 * m][p] = __builtin_amdgcn_perm(w[2 * p + 1][m], w[2 * p][m], 0x05040100u);
      f[2 * m + 1][p] = __builtin_amdgcn_perm(w[2 * p + 1][m], w[2 * p][m], 0x07060302u);
    }
}
DI void kfold_store(const f32x4 (&d)[8], float* slot, int lane) {
  const bool lo = (lane & 8) == 0;
  float o[8];
#pragma unroll
  for (int j = 0; j < 8; ++j) {
    float x = lo ? d[j][0] : d[j][3], y = lo ? d[j][2] : d[j][1];
    o[j] = x + dppf<0x128>(y);
  }
  *(f32x4*)(slot + lane * 8) = f32x4{o[0], o[1], o[2], o[3]};
  *(f32x4*)(slot + lane * 8 + 4) = f32x4{o[4], o[5], o[6], o[7]};
}
DI float sum8(const float* p, int stride) {
  return ((p[0] + p[stride]) + (p[2 * stride] + p[3 * stride])) + ((p[4 * stride] + p[5 * stride]) + (p[6 * stride] + p[7 * stride]));
}
DI f32x2 sum8x2(const float* p, int stride) {
  auto L = [&](int i) { return *(const f32x2*)(p + i * stride); };
  return ((L(0) + L(1)) + (L(2) + L(3))) + ((L(4) + L(5)) + (L(6) + L(7)));
}
DI v4u canon4(f32x4 s) { return v4u{canon(fbits(s[0])), canon(fbits(s[1])), canon(fbits(s[2])), canon(fbits(s[3]))}; }
DI f32x4 asf(v4u x) { return __builtin_bit_cast(f32x4, x); }
DI v4u silu8(v4u c) {
  v4u r;
#pragma unroll
  for (int m = 0; m < 4; ++m) {
    float a = bflo(c[m]), b = bfhi(c[m]);
    a = a * rcp(1.f + ex2(-a * LOG2E)); b = b * rcp(1.f + ex2(-b * LOG2E));
    r[m] = pk2(a, b);
  }
  return r;
}

struct Args {
  const u16 *x, *kc, *vc, *wq, *wo, *wg, *wu, *wd, *c, *wmod;
  u16* out;
  char* ws;
};

#define STAMP(i) do { } while (0)

__global__ void __launch_bounds__(768, 3) mega(Args a) {
  __shared__ __attribute__((aligned(16))) unsigned cnt[C_N];
  __shared__ __attribute__((aligned(16))) unsigned qs[8 * 68];
  __shared__ __attribute__((aligned(16))) float redC[8 * 512];
  __shared__ __attribute__((aligned(16))) float mlw[8 * 16];
  __shared__ __attribute__((aligned(16))) float mlf[16];
  __shared__ __attribute__((aligned(16))) float redO[8 * 1024];
  __shared__ __attribute__((aligned(16))) float redT[8 * 1152];
  __shared__ __attribute__((aligned(16))) float redU[8 * 512];

  const int bid = blockIdx.x, lane = threadIdx.x & 63;
  const int w = __builtin_amdgcn_readfirstlane(threadIdx.x >> 6);
  const int xc = bid & 7, jx = bid >> 3;
  const int ab = xc, akvh = jx >> 3, as = jx & 7;
  const int grp = ab * 4 + akvh;
  const bool typeB = jx >= 16;
  const int jb = jx - 16;
  const int g = lane >> 4, n = lane & 15;
  const int arow = (n >> 1) * 2048 + (n & 1) * 64 + g * 16;
  const bool doT3 = jx < 14;
  const rsrc_t rws = mk(a.ws, WS_BYTES);
  if (threadIdx.x < C_N) cnt[threadIdx.x] = 0;

  if (w < 8) {
    if (w == 0) STAMP(0);
    {
      const v4u S = {SENT, SENT, SENT, SENT};
      st_sc(S, rws, lane * 16, WS_ARENA + bid * ARENA_PER + w * 1024);
      st_sc(S, rws, lane * 16, WS_ARENA + bid * ARENA_PER + (w < 3 ? 8 + w : w) * 1024);
    }
    const rsrc_t rk = mk(a.kc, 64u << 20), rv = mk(a.vc, 64u << 20);
    const int kvbase = (grp * 8 + as) * (256 << 10) + w * (32 << 10);
    const int koff = kvbase + ((lane >> 2) & 3) * 256 + g * 64 + (lane & 3) * 16;
    const int voff = kvbase + lane * 16;
    const bool l0 = lane & 1, l1 = lane & 2;
    const int ta = xc * 14 + jx;
    const int hp = 2 * xc + (jb >> 3), cg = jb & 7;
    const int td = xc * 14 + jb;
    const bool doT4 = typeB && jb < 14;
    v4u k[32];
    v4u wt[16];
    v4u v[32];
    f32x4 S[8];
    v4u qf[4];
    auto ktr = [&](int t) {
#pragma unroll
      for (int m = 0; m < 4; ++m) {
        unsigned r0 = k[4 * t][m], r1 = k[4 * t + 1][m], r2 = k[4 * t + 2][m], r3 = k[4 * t + 3][m];
        const unsigned d0 = dppu<0xB1>(r0), d1 = dppu<0xB1>(r1), d2 = dppu<0xB1>(r2), d3 = dppu<0xB1>(r3);
        const unsigned s0 = l0 ? d1 : r0, s1 = l0 ? r1 : d0, s2 = l0 ? d3 : r2, s3 = l0 ? r3 : d2;
        const unsigned e0 = dppu<0x4E>(s0), e1 = dppu<0x4E>(s1), e2 = dppu<0x4E>(s2), e3 = dppu<0x4E>(s3);
        k[4 * t][m] = l1 ? e2 : s0; k[4 * t + 2][m] = l1 ? s2 : e0;
        k[4 * t + 1][m] = l1 ? e3 : s1; k[4 * t + 3][m] = l1 ? s3 : e1;
      }
      asm volatile("" : "+v"(k[4 * t]), "+v"(k[4 * t + 1]), "+v"(k[4 * t + 2]), "+v"(k[4 * t + 3]));
      SB0();
    };
    auto vld = [&](int i) { SB0(); v[i] = ld_nt(rv, voff, i * 1024); SB0(); };
    SB0();
#pragma unroll
    for (int i = 0; i < 32; ++i) {
      k[i] = ld_nt(rk, koff, i * 1024);
      SB0();
      if (i == KWIN - 1) {
        asm volatile("s_waitcnt lgkmcnt(0)\n\ts_barrier\n\ts_waitcnt vmcnt(%0)" :: "n"(KWIN) : "memory");
        lds_arrive(&cnt[C_RESET], lane);
      }
      if (i >= KWIN - 1 && i < 31) VMWAIT(KWIN - 1);
      SB0();
      if (i >= KWIN + 2 && ((i - KWIN + 2) & 3) == 0 && i < 31) ktr((i - KWIN + 2) / 4 - 1);
    }
    constexpr int KDONE = (30 - KWIN + 2) / 4;
    if (w == 0) STAMP(1);
    lds_wait<2>(&cnt[C_QG], 4);
    if (w == 0) STAMP(2);
#pragma unroll
    for (int i = 0; i < 4; ++i) qf[i] = *(const v4u*)(qs + (n & 7) * 68 + g * 16 + i * 4);
#pragma unroll
    for (int u = 0; u < 8; ++u) {
      if (u >= KDONE) ktr(u);
      f32x4 s = {0.f, 0.f, 0.f, 0.f};
#pragma unroll
      for (int i = 0; i < 4; ++i) s = mfma(k[4 * u + i], qf[i], s);
      S[u] = s;
      if (u == 1) { vld(0); vld(1); vld(2); }
      if (u == 3) vld(3);
      if (u == 6) vld(4);
      SB0();
    }
    float mw = -INFINITY;
#pragma unroll
    for (int t = 0; t < 8; ++t) {
      unsigned p0 = pk2(S[t][0], S[t][1]), p1 = pk2(S[t][2], S[t][3]);
      const float sc = 0.08838834764831845f;
      p0 = pk2(bflo(p0) * sc, bfhi(p0) * sc); p1 = pk2(bflo(p1) * sc, bfhi(p1) * sc);
      S[t] = f32x4{bflo(p0), bfhi(p0), bflo(p1), bfhi(p1)};
      mw = fmaxf(mw, fmaxf(fmaxf(S[t][0], S[t][1]), fmaxf(S[t][2], S[t][3])));
      if (t == 0 || t == 3 || t == 6) vld(5 + t / 3);
    }
    mw = max32(max16(mw));
    float lw = 0.f;
    const float nm = -mw * LOG2E;
    v4u P[4];
#pragma unroll
    for (int t = 0; t < 8; ++t) {
      float e0 = ex2(fmaf(S[t][0], LOG2E, nm)), e1 = ex2(fmaf(S[t][1], LOG2E, nm)), e2 = ex2(fmaf(S[t][2], LOG2E, nm)), e3 = ex2(fmaf(S[t][3], LOG2E, nm));
      lw += (e0 + e1) + (e2 + e3);
      unsigned p0 = pk2(e0, e1), p1 = pk2(e2, e3);
      asm volatile("" : "+v"(p0), "+v"(p1));
      P[t >> 1][(t & 1) * 2] = p0; P[t >> 1][(t & 1) * 2 + 1] = p1;
      if (t == 1 || t == 4 || t == 7) vld(8 + t / 3);
    }
    lw = add32(add16(lw));
    {
      if (lane < 8) *(f32x2*)(mlw + w * 16 + lane * 2) = f32x2{mw, lw};
      CBAR();
      unsigned old = 0;
      if (lane == 0) old = __hip_atomic_fetch_add(&cnt[C_MLW], 1u, __ATOMIC_RELAXED, __HIP_MEMORY_SCOPE_WORKGROUP);
      old = __builtin_amdgcn_readfirstlane(old);
      CBAR();
      if (old == 7) {
        f32x2 r = *(const f32x2*)(mlw + lane * 2);
        float M = max32(max16(max8(r[0])));
        float L = add32(add16(add8(r[1] * ex2((r[0] - M) * LOG2E))));
        if (lane < 8) *(f32x2*)(mlf + lane * 2) = f32x2{M, L};
        lds_arrive(&cnt[C_MLF], lane);
      }
    }
    if (w == 0) STAMP(3);
    f32x4 O[8];
#pragma unroll
    for (int j = 0; j < 8; ++j) O[j] = f32x4{0.f, 0.f, 0.f, 0.f};
    v4u sbcb[4];
    const int auxv = typeB ? (n >> 1) * 64 : arow;
    const int auxb = typeB ? WS_ML + (2 * hp + (w >> 2)) * 512 : WS_SB + w * 256;
    const int auxh = typeB ? 32 : 16384, auxl = typeB ? 16 : 128;
    const rsrc_t rw0 = mk(typeB ? a.wo : (ta >= 56 ? a.wu : a.wg), typeB ? (8u << 20) : (doT3 ? (7u << 20) : 0u));
    const int wstride = typeB ? 2048 : 7168;
    const int woff = typeB ? ((2 * hp + (w >> 2)) * 128 + (w & 3) * 32 + g * 8) * 2048 + cg * 256 + n * 16
                           : (w * 128 + (n >> 3) * 32 + g * 8) * 7168 + (ta % 56) * 128 + (n & 7) * 16;
    auto pv = [&](int blk) {
#pragma unroll
      for (int m = 0; m < 4; ++m) {
        v4u f0, f1;
#pragma unroll
        for (int p = 0; p < 4; ++p) {
          f0[p] = __builtin_amdgcn_perm(v[8 * blk + 2 * p + 1][m], v[8 * blk + 2 * p][m], 0x05040100u);
          f1[p] = __builtin_amdgcn_perm(v[8 * blk + 2 * p + 1][m], v[8 * blk + 2 * p][m], 0x07060302u);
        }
        O[2 * m] = mfma(f0, P[blk], O[2 * m]);
        O[2 * m + 1] = mfma(f1, P[blk], O[2 * m + 1]);
        SB0();
      }
    };
    static_assert(VWIN >= 2 && VWIN <= 5, "");
#pragma unroll
    for (int i = 11; i < 32; ++i) {
      VMWAIT(VWIN - 1);
      vld(i);
      if (i == 12) pv(0);
      if (i == 15 + VWIN) pv(1);
      if (i == 23 + VWIN) pv(2);
    }
    lds_arrive(&cnt[C_VI], lane);
    float fs;
    {
      lds_wait<1>(&cnt[C_MLF], 1);
      f32x2 ml = *(const f32x2*)(mlf + (n & 7) * 2);
      fs = ex2((mw - ml[0]) * LOG2E) * rcp(ml[1]);
      asm volatile("" : "+v"(fs));
    }
    lds_wait<1>(&cnt[C_VI], 8);
    SB0();
#pragma unroll
    for (int i = 0; i < 4; ++i) sbcb[i] = ld_sc(rws, auxv, auxb + (i >> 1) * auxh + (i & 1) * auxl);
#pragma unroll
    for (int i = 0; i < WEARLY; ++i) wt[i] = ld_nt(rw0, woff, i * wstride);
    SB0();
    pv(3);
    if (w == 0) STAMP(4);
    if (w == 7) STAMP(26);
    {
      if (n < 8) {
        float* dst = redO + w * 1024 + n * 128 + g * 32;
#pragma unroll
        for (int r = 0; r < 4; ++r) {
          *(f32x4*)(dst + r * 8) = f32x4{O[0][r] * fs, O[1][r] * fs, O[2][r] * fs, O[3][r] * fs};
          *(f32x4*)(dst + r * 8 + 4) = f32x4{O[4][r] * fs, O[5][r] * fs, O[6][r] * fs, O[7][r] * fs};
        }
      }
      lds_arrive(&cnt[C_O], lane);
      lds_wait<0>(&cnt[C_O], 8);
      const f32x2 s = sum8x2(redO + w * 128 + lane * 2, 1024);
      st_sc1(canon(pk2(s[0], s[1])), rws, g * 4096 + n * 4, WS_OP + (akvh * 8 + w) * 16384 + (as >> 1) * 1024 + ab * 128 + (as & 1) * 64);
    }
    SB0();
#pragma unroll
    for (int i = WEARLY; i < 8; ++i) wt[i] = ld_nt(rw0, woff, i * wstride);
    if (doT3) {
#pragma unroll
      for (int i = 0; i < 8; ++i) wt[8 + i] = ld_nt(rw0, woff, (64 + i) * 7168);
    }
    SB0();
    if (w == 0) STAMP(5);
    CBAR();
#pragma unroll 1
    for (int i = 0; i < GCAP; ++i) {
      unsigned m = max(max(umax4(sbcb[0]), umax4(sbcb[1])), max(umax4(sbcb[2]), umax4(sbcb[3])));
      if (__builtin_amdgcn_ballot_w64(m == SENT) == 0) break;
      __builtin_amdgcn_s_sleep(1);
      CBAR();
#pragma unroll
      for (int j = 0; j < 4; ++j) sbcb[j] = ld_sc(rws, auxv, auxb + (j >> 1) * auxh + (j & 1) * auxl);
    }
    CBAR();
    v4u f[8];
    if (typeB) {
      float F[4];
      {
        float M = -INFINITY, tot = 0.f;
#pragma unroll
        for (int p = 0; p < 4; ++p) M = fmaxf(M, fmaxf(bitsf(sbcb[p][0]), bitsf(sbcb[p][2])));
#pragma unroll
        for (int p = 0; p < 4; ++p) {
          const float w0 = bitsf(sbcb[p][1]) * ex2((bitsf(sbcb[p][0]) - M) * LOG2E), w1 = bitsf(sbcb[p][3]) * ex2((bitsf(sbcb[p][2]) - M) * LOG2E);
          tot += w0 + w1;
          F[p] = l0 ? w1 : w0;
        }
        tot = rcp(tot);
#pragma unroll
        for (int p = 0; p < 4; ++p) F[p] *= tot;
      }
      asm volatile("" : "+v"(F[0]), "+v"(F[1]), "+v"(F[2]), "+v"(F[3]));
      tr8((const v4u(&)[8])wt[0], f);
      PIN8(f);
      if (w == 0) STAMP(27);
      v4u oa[4];
      poll4<4>(oa, rws, n * 64 + g * 16, WS_OP + (2 * hp + (w >> 2)) * 16384 + (w & 3) * 4096, 1024);
      if (w == 0) STAMP(6);
      v4u oo;
#pragma unroll
      for (int m = 0; m < 4; ++m) {
        float lo = bflo(oa[0][m]) * F[0], hi = bfhi(oa[0][m]) * F[0];
#pragma unroll
        for (int p = 1; p < 4; ++p) { lo = fmaf(bflo(oa[p][m]), F[p], lo); hi = fmaf(bfhi(oa[p][m]), F[p], hi); }
        oo[m] = pk2(lo + dppf<0xB1>(lo), hi + dppf<0xB1>(hi));
      }
      f32x4 d[8];
      float* dst = redT + w * 1152 + g * 288 + n;
#pragma unroll
      for (int j = 0; j < 8; ++j) d[j] = mfma(oo, f[j], f32x4{0.f, 0.f, 0.f, 0.f});
#pragma unroll
      for (int j = 0; j < 8; ++j) { dst[j * 16] = d[j][0]; dst[144 + j * 16] = d[j][2]; }
      lds_arrive(&cnt[C_T1], lane);
      lds_wait<0>(&cnt[C_T1], 8);
      {
        const f32x2 s = sum8x2(redT + w * 144 + ((lane >> 1) & 7) * 16 + g * 4 + (lane & 1) * 2, 1152);
        st_sc2(v2u{canon(fbits(s[0])), canon(fbits(s[1]))}, rws, g * 2048 + n * 8, WS_XP + (w * 32 + cg * 4) * 2048 + hp * 128);
      }
      if (w == 0) STAMP(7);
      if (doT4) {
        const int ks = td >> 4, c16 = td & 15;
        lds_wait<1>(&cnt[C_T2], 2);
        const rsrc_t rw = mk(a.wd, 7u << 20);
        const int off = (ks * 512 + w * 64 + (n >> 3) * 32 + g * 8) * 2048 + c16 * 128 + (n & 7) * 16;
#pragma unroll
        for (int i = 0; i < 8; ++i) wt[i] = ld_nt(rw, off, i * 2048);
        tr8((const v4u(&)[8])wt[0], f);
        PIN8(f);
        if (w == 0) STAMP(12);
        v4u gu[2];
        poll4<2>(gu, rws, n * 64 + g * 16, WS_GU + (ks * 8 + w) * 1024, 56 * 1024);
        if (w == 0) STAMP(13);
        v4u hh;
#pragma unroll
        for (int m = 0; m < 4; ++m) hh[m] = pk2(bflo(gu[0][m]) * bflo(gu[1][m]), bfhi(gu[0][m]) * bfhi(gu[1][m]));
#pragma unroll
        for (int j = 0; j < 8; ++j) d[j] = mfma(hh, f[j], f32x4{0.f, 0.f, 0.f, 0.f});
        kfold_store(d, redU + w * 512, lane);
        lds_arrive(&cnt[C_T4], lane);
        lds_wait<0>(&cnt[C_T4], 8);
        const float t = sum8(redU + w * 64 + lane, 512);
        st_sc1(canon(fbits(t)), rws, (lane >> 5) * 1024 + (lane & 31) * 4, WS_DP + (w * 32 + c16 * 2) * 1024 + ks * 128);
        if (w == 0) STAMP(14);
      }
    } else if (doT3) {
      v4u f1[8];
      tr8((const v4u(&)[8])wt[0], f);
      PIN8(f);
      SB0();
      tr8((const v4u(&)[8])wt[8], f1);
      PIN8(f1);
      SB0();
      f32x4 d[8];
#pragma unroll
      for (int j = 0; j < 8; ++j) d[j] = mfma(sbcb[1], f1[j], mfma(sbcb[0], f[j], f32x4{0.f, 0.f, 0.f, 0.f}));
      kfold_store(d, redT + w * 512, lane);
#pragma unroll
      for (int j = 0; j < 8; ++j) d[j] = mfma(sbcb[3], f1[j], mfma(sbcb[2], f[j], f32x4{0.f, 0.f, 0.f, 0.f}));
      kfold_store(d, redT + 4096 + w * 512, lane);
      lds_arrive(&cnt[C_C12], lane);
      lds_wait<1>(&cnt[C_C12], 8);
      float c2 = sum8(redT + w * 64 + lane, 512), c1 = sum8(redT + 4096 + w * 64 + lane, 512);
      asm volatile("" : "+v"(c1), "+v"(c2));
      if (w == 0) STAMP(9);
      lds_wait<1>(&cnt[C_PRE], 1);
      v4u xr[2];
      v2u st;
      {
        const int sb0 = WS_X1 + xc * REPX;
        CBAR();
#pragma unroll 1
        for (int i = 0; i < GCAP; ++i) {
          xr[0] = ld_sc(rws, arow, sb0 + w * 256); xr[1] = ld_sc(rws, arow, sb0 + w * 256 + 128);
          st = ld_sc2(rws, (lane & 31) * 8, sb0 + 16384 + w * 256);
          unsigned m = max(max(umax4(xr[0]), umax4(xr[1])), max(st.x, st.y));
          if (__builtin_amdgcn_ballot_w64(m == SENT) == 0) break;
          __builtin_amdgcn_s_sleep(1);
          CBAR();
        }
        CBAR();
      }
      if (w == 0) STAMP(10);
#pragma unroll
      for (int j = 0; j < 8; ++j) d[j] = mfma(xr[1], f1[j], mfma(xr[0], f[j], f32x4{0.f, 0.f, 0.f, 0.f}));
      float mean, rden;
      {
        const float mb = bitsf(st.x);
        const float mu = sum32(mb) * (1.f / 32.f);
        const float var = sum32(fmaf((mb - mu) * 32.f, mb - mu, bitsf(st.y))) * (1.f / 1024.f);
        mean = rbf(mu);
        rden = rcp(rbf(__builtin_amdgcn_sqrtf(rbf(rbf(var) + 1e-5f))));
      }
      kfold_store(d, redU + w * 512, lane);
      lds_arrive(&cnt[C_T3], lane);
      c2 = fmaf(-mean * rden, c1, c2);
      lds_wait<0>(&cnt[C_T3], 8);
      float t = fmaf(sum8(redU + w * 64 + lane, 512), rden, c2);
      if (ta < 56) { t = rbf(t); t = t > 0.f ? t : t * 0.f; }
      const unsigned u = canon(pk2(t, dppf<0xB1>(t)));
      if (!l0) st_sc1(u, rws, lane * 2, WS_GU + ta * 1024 + w * 128);
      if (w == 0) STAMP(11);
    }
  } else {
    const int cw = w - 8;
    __builtin_amdgcn_s_setprio(2);
    const u64 ep = *(const __attribute__((address_space(4))) u64*)(uintptr_t)a.ws;
    const u64 tag = ep + 0x9E3779B97F4A7C15ull;
    unsigned tlo = (unsigned)tag, thi = (unsigned)(tag >> 32);
    asm volatile("" : "+s"(tlo), "+s"(thi));
    const rsrc_t rc = mk(a.c, 16384), rm = mk(a.wmod, 12u << 20), rx = mk(a.x, 16384);
    const int wlane_r = (n >> 3) * 32 + g * 8, wlane_c = (n & 7) * 16;
    const int rcb = 2 * jb + cw, rr = xc * 32 + rcb, col = lane & 31, half = lane >> 5;
    unsigned xo = 0;
    if (cw >= 2) {
      const int h = cw - 2;
      const int ct = bid & 31, ks = bid >> 5;
      const int qct = bid & 63, qks = bid >> 6;
      v4u wm[8], wq0[8], wq1[8];
      const v4u cv = ld_pl(rc, arow, (ks * 128 + h * 64) * 2);
      {
        const int off = (ks * 128 + h * 64 + wlane_r) * 12288 + ct * 128 + wlane_c;
#pragma unroll
        for (int i = 0; i < 8; ++i) wm[i] = ld_nt(rm, off, i * 12288);
      }
      SB0();
      asm volatile("s_waitcnt lgkmcnt(0)\n\ts_barrier" ::: "memory");
      const v4u ca = silu8(cv);
      v4u f[8];
      f32x4 d[8];
      tr8(wm, f);
#pragma unroll
      for (int j = 0; j < 8; ++j) d[j] = mfma(ca, f[j], f32x4{0.f, 0.f, 0.f, 0.f});
      kfold_store(d, redC + h * 512, lane);
      lds_arrive(&cnt[C_M1], lane);
      lds_wait<0>(&cnt[C_M1], 2);
      {
        const float* p = redC + h * 256 + lane * 2;
        const f32x2 s0 = *(const f32x2*)p + *(const f32x2*)(p + 512), s1 = *(const f32x2*)(p + 128) + *(const f32x2*)(p + 640);
        st_sc(v4u{fbits(s0[0]), tlo, fbits(s0[1]), thi}, rws, lane * 16, WS_MPA + (ct * 8 + ks) * MPA_SLOT + h * 2048);
        st_sc(v4u{fbits(s1[0]), tlo, fbits(s1[1]), thi}, rws, lane * 16, WS_MPA + (ct * 8 + ks) * MPA_SLOT + h * 2048 + 1024);
      }
      lds_arrive(&cnt[C_M1S], lane);
      if (cw == 2) STAMP(16);
      SB0();
      {
        const rsrc_t rq = mk(a.wq, 8u << 20);
        const int off = (qks * 256 + h * 128 + wlane_r) * 8192 + qct * 128 + wlane_c;
#pragma unroll
        for (int i = 0; i < 16; ++i) {
          if (i >= 3) VMWAIT(2);
          SB0();
          if (i < 8) wq0[i] = ld_nt(rq, off, i * 8192); else wq1[i - 8] = ld_nt(rq, off, (56 + i) * 8192);
          SB0();
        }
      }
      v4u f2[8];
      tr8(wq0, f);
      tr8(wq1, f2);
      PIN8(f);
      PIN8(f2);
      if (cw == 2) STAMP(28);
      lds_wait<1>(&cnt[C_R1], 1);
      v4u ha[4];
      pollt(ha, rws, (n >> 1) * 4096 + (n & 1) * 128 + g * 32, WS_H + xc * HREP + (qks * 256 + h * 128) * 4, 256, 16, tlo, thi);
      if (cw == 2) STAMP(19);
      const v4u a0 = {ha[0].x, ha[0].z, ha[1].x, ha[1].z}, a1 = {ha[2].x, ha[2].z, ha[3].x, ha[3].z};
#pragma unroll
      for (int j = 0; j < 8; ++j) d[j] = mfma(a1, f2[j], mfma(a0, f[j], f32x4{0.f, 0.f, 0.f, 0.f}));
      kfold_store(d, redC + 1024 + h * 512, lane);
      lds_arrive(&cnt[C_Q], lane);
      lds_wait<0>(&cnt[C_Q], 2);
      {
        f32x4 s = *(const f32x4*)(redC + 1024 + h * 256 + lane * 4) + *(const f32x4*)(redC + 1536 + h * 256 + lane * 4);
        lds_wait<1>(&cnt[C_GO], 1);
        st_sc(canon4(s), rws, g * 65536 + n * 16, WS_QP + h * 4 * 65536 + (qct >> 4) * 16384 + qks * 4096 + (qct & 15) * 256);
      }
      lds_arrive(&cnt[C_QS], lane);
      if (cw == 2) STAMP(20);
    } else {
      v4u xr[2];
      unsigned xp = 0;
      if (typeB) xo = ld_pl1(rx, (col >> 1) * 4, xc * 2048 + rcb * 64);
      if (cw == 1) {
        xr[0] = ld_pl(rx, lane * 16, xc * 2048); xr[1] = ld_pl(rx, lane * 16, xc * 2048 + 1024);
        xp = ld_pl1(rx, n * 4, xc * 2048 + jx * 64);
      }
      asm volatile("s_waitcnt lgkmcnt(0)\n\ts_barrier" ::: "memory");
      if (cw == 0) {
        lds_wait<1>(&cnt[C_RESET], 8);
        if (lane < 8) st_sc2(v2u{tlo, thi}, rws, lane * FLAG_REP, WS_FLAGS + bid * 8);
        STAMP(15);
        CBAR();
#pragma unroll 1
        for (int i = 0; i < GCAP; ++i) {
          __builtin_amdgcn_s_sleep(12);
          v4u f0 = ld_sc(rws, lane * 16, WS_FLAGS + xc * FLAG_REP), f1 = ld_sc(rws, lane * 16, WS_FLAGS + xc * FLAG_REP + 1024);
          const unsigned bad = (f0.x ^ tlo) | (f0.y ^ thi) | (f0.z ^ tlo) | (f0.w ^ thi) | (f1.x ^ tlo) | (f1.y ^ thi) | (f1.z ^ tlo) | (f1.w ^ thi);
          if (__builtin_amdgcn_ballot_w64(bad != 0) == 0) break;
          CBAR();
        }
        lds_arrive(&cnt[C_GO], lane);
        if (bid == 0 && lane == 0) st_sc2(v2u{tlo, thi}, rws, 0, WS_EPOCH);
        STAMP(17);
      } else {
        float ln0, ln1;
        {
          const float x0 = bitsf(__builtin_amdgcn_readfirstlane(xr[0][0] << 16));
          float s = 0.f, q = 0.f;
#pragma unroll
          for (int i = 0; i < 8; ++i) {
            const float d0 = bflo(xr[i >> 2][i & 3]) - x0, d1 = bfhi(xr[i >> 2][i & 3]) - x0;
            s += d0 + d1; q = fmaf(d0, d0, fmaf(d1, d1, q));
          }
          s = wave_sum(s); q = wave_sum(q);
          const float md = s * (1.f / 1024.f);
          const float var = fmaxf(q * (1.f / 1024.f) - md * md, 0.f);
          const float mean = rbf(x0 + md);
          const float den = rbf(__builtin_sqrtf(rbf(rbf(var) + 1e-5f)));
          ln0 = rbf(rbf(bflo(xp) - mean) / den); ln1 = rbf(rbf(bfhi(xp) - mean) / den);
          asm volatile("" : "+v"(ln0), "+v"(ln1));
        }
        lds_wait<1>(&cnt[C_M1S], 2);
        v4u mp[4];
        pollt(mp, rws, g * MPA_SLOT + n * 16, WS_MPA + (jx >> 1) * 8 * MPA_SLOT + xc * 512 + (jx & 1) * 256, 128 * MPA_SLOT, 4 * MPA_SLOT, tlo, thi);
        const float sa0 = rbf(add32(add16(bitsf(mp[0].x) + bitsf(mp[1].x)))), sa1 = rbf(add32(add16(bitsf(mp[0].z) + bitsf(mp[1].z))));
        const float ca0 = rbf(add32(add16(bitsf(mp[2].x) + bitsf(mp[3].x)))), ca1 = rbf(add32(add16(bitsf(mp[2].z) + bitsf(mp[3].z))));
        const unsigned u = pk2(rbf(ln0 * rbf(1.f + ca0)) + sa0, rbf(ln1 * rbf(1.f + ca1)) + sa1);
        st_sc(v4u{dppu<0xA0>(u), tlo, dppu<0xF5>(u), thi}, rws, (g * 2 + (n & 1)) * HREP + (n >> 1) * 16, WS_H + xc * 4096 + jx * 128);
        lds_arrive(&cnt[C_R1], lane);
        STAMP(18);
      }
    }
    lds_wait<1>(&cnt[C_QS], 2);
    {
      v4u qp[4];
      poll4<4>(qp, rws, lane * 16, WS_QP + grp * 16384 + cw * 1024, 4096);
      f32x4 s = (asf(qp[0]) + asf(qp[1])) + (asf(qp[2]) + asf(qp[3]));
      *(v2u*)(qs + (cw * 2 + (lane >> 5)) * 68 + (lane & 31) * 2) = v2u{pk2(s[0], s[1]), pk2(s[2], s[3])};
      lds_arrive(&cnt[C_QG], lane);
      if (cw == 0) STAMP(21);
    }
    __builtin_amdgcn_s_setprio(0);
    lds_wait<2>(&cnt[C_QG], 4);
    {
      const int ct = bid & 63, ks = bid >> 6;
      v4u w0[8];
      const int r0 = ks * 256 + cw * 64;
      const v4u c0 = ld_pl(rc, arow, r0 * 2);
      const int off = (r0 + wlane_r) * 12288 + 4096 + ct * 128 + wlane_c;
#pragma unroll
      for (int i = 0; i < 8; ++i) w0[i] = ld_nt(rm, off, i * 12288);
      SB0();
      const v4u a0 = silu8(c0);
      v4u f[8];
      tr8(w0, f);
      f32x4 d[8];
#pragma unroll
      for (int j = 0; j < 8; ++j) d[j] = mfma(a0, f[j], f32x4{0.f, 0.f, 0.f, 0.f});
      kfold_store(d, redC + 2048 + cw * 512, lane);
      lds_arrive(&cnt[C_M2], lane);
      lds_wait<1>(&cnt[C_M2], 4);
      const float* src = redC + 2048 + (lane >> 5) * 1024 + cw * 128 + (lane & 31) * 4;
      f32x4 s = *(const f32x4*)src + *(const f32x4*)(src + 512);
      s = f32x4{add32(s[0]), add32(s[1]), add32(s[2]), add32(s[3])};
      if (lane < 32) st_sc(canon4(s), rws, g * 65536 + (n >> 3) * 2048 + (n & 7) * 16, WS_MPB + cw * 131072 + (ct & 15) * 4096 + (ct >> 4) * 512 + ks * 128);
      if (cw == 0) STAMP(22);
    }
    if (cw == 3) {
      lds_wait<8>(&cnt[C_MLF], 1);
      if (lane < 8) {
        const f32x2 ml = *(const f32x2*)(mlf + lane * 2);
        st_sc2(v2u{canon(fbits(ml[0])), canon(fbits(ml[1]))}, rws, lane * 512, WS_ML + akvh * 4096 + ab * 64 + as * 8);
      }
      STAMP(23);
    }
    __builtin_amdgcn_s_setprio(2);
    if (doT3 && cw == 0) {
      lds_wait<8>(&cnt[C_O], 8);
      v4u t[2];
      poll4<2, 4>(t, rws, lane * 16, WS_XP + (bid * 2 + 1) * 2048, 1024);
      lds_arrive(&cnt[C_PRE], lane);
      STAMP(29);
    }
    if (typeB && cw < 2) {
      float ga, gb, cb1;
      {
        unsigned mp[8];
        CBAR();
#pragma unroll 1
        for (int i = 0; i < GCAP; ++i) {
          unsigned m = 0;
#pragma unroll
          for (int j = 0; j < 8; ++j) { mp[j] = ld_sc1(rws, half * 256 + col * 4, WS_MPB + rr * 2048 + (j >> 1) * 512 + (j & 1) * 128); m = max(m, mp[j]); }
          if (__builtin_amdgcn_ballot_w64(m == SENT) == 0) break;
          __builtin_amdgcn_s_sleep(16);
          CBAR();
        }
        CBAR();
        ga = rbf(add32(bitsf(mp[0]) + bitsf(mp[1])));
        const float sb = add32(bitsf(mp[2]) + bitsf(mp[3]));
        cb1 = rbf(1.f + rbf(add32(bitsf(mp[4]) + bitsf(mp[5]))));
        gb = rbf(add32(bitsf(mp[6]) + bitsf(mp[7])));
        const float z = half ? cb1 : sb;
        const unsigned u = canon(pk2(z, dppf<0xB1>(z)));
        if (!(lane & 1)) st_sc1(u, rws, half * 16384 + col * 2, WS_SB + xc * 2048 + rcb * 64);
        if (cw == 0) STAMP(24);
      }
      const float xv = (lane & 1) ? bfhi(xo) : bflo(xo);
      lds_wait<4>(&cnt[C_T1], 8);
      float x1;
      {
        unsigned xp[8];
        poll1<8>(xp, rws, half * 1024 + (col & 7) * 16 + (col >> 3) * 4, WS_XP + rr * 2048, 128);
        const float s = add32(((bitsf(xp[0]) + bitsf(xp[1])) + (bitsf(xp[2]) + bitsf(xp[3]))) + ((bitsf(xp[4]) + bitsf(xp[5])) + (bitsf(xp[6]) + bitsf(xp[7]))));
        x1 = rbf(xv + rbf(ga * rbf(s)));
        const unsigned u = canon(pair_pack(x1 * cb1));
        const int vo = ((lane & 1) + 2 * half) * REPX + (col >> 1) * 4;
        st_sc1(u, rws, vo, WS_X1 + xc * 2048 + rcb * 64);
        st_sc1(u, rws, vo, WS_X1 + 4 * REPX + xc * 2048 + rcb * 64);
        const float x0 = bitsf(__builtin_amdgcn_readfirstlane(fbits(x1)));
        const float e = x1 - x0;
        const float sm = sum32(e), sq = sum32(e * e);
        const float md = sm * (1.f / 32.f);
        if (lane < 8) st_sc2(v2u{canon(fbits(x0 + md)), canon(fbits(fmaxf(sq - sm * md, 0.f)))}, rws, lane * REPX, WS_X1 + 16384 + xc * 256 + rcb * 8);
        lds_arrive(&cnt[C_T2], lane);
        if (cw == 0) STAMP(8);
      }
      if (jb < 14) lds_wait<1>(&cnt[C_T4], 8);
      {
        unsigned dp[4];
        CBAR();
#pragma unroll 1
        for (int i = 0; i < GCAP; ++i) {
#pragma unroll
          for (int j = 0; j < 3; ++j) dp[j] = ld_sc1(rws, half * 512 + col * 4, WS_DP + rr * 1024 + j * 128);
          dp[3] = ld_sc1(rws, half * 384 + col * 4, WS_DP + rr * 1024 + 384);
          const unsigned m = max(max(dp[0], dp[1]), max(dp[2], dp[3]));
          if (__builtin_amdgcn_ballot_w64(m == SENT) == 0) break;
          __builtin_amdgcn_s_sleep(1);
          CBAR();
        }
        CBAR();
        const float s = add32((bitsf(dp[0]) + bitsf(dp[1])) + (bitsf(dp[2]) + (half ? 0.f : bitsf(dp[3]))));
        const float o = x1 + rbf(gb * rbf(s));
        const unsigned u = pk2(o, dppf<0xB1>(o));
        if (lane < 32 && !(lane & 1)) *(unsigned*)(a.out + xc * 1024 + rcb * 32 + col) = u;
        if (cw == 0) STAMP(25);
      }
    }
  }
}

extern "C" void kernel_launch(void* const* d_in, const int* in_sizes, int n_in, void* d_out, int out_size, void* d_ws, size_t ws_size, hipStream_t stream) {
  Args a;
  a.x = (const u16*)d_in[0]; a.kc = (const u16*)d_in[1]; a.vc = (const u16*)d_in[2]; a.wq = (const u16*)d_in[3]; a.wo = (const u16*)d_in[4];
  a.wg = (const u16*)d_in[6]; a.wu = (const u16*)d_in[7]; a.wd = (const u16*)d_in[8]; a.c = (const u16*)d_in[9]; a.wmod = (const u16*)d_in[10];
  a.out = (u16*)d_out; a.ws = (char*)d_ws;
  mega<<<256, 768, 0, stream>>>(a);
}
```

```cpp
#include <hip/hip_runtime.h>
#include <stdint.h>

typedef unsigned short u16;
typedef unsigned long long u64;
typedef unsigned v4u __attribute__((ext_vector_type(4)));
typedef unsigned v2u __attribute__((ext_vector_type(2)));
typedef float f32x4 __attribute__((ext_vector_type(4)));
typedef float f32x2 __attribute__((ext_vector_type(2)));
typedef __bf16 bf16x8 __attribute__((ext_vector_type(8)));
typedef __bf16 bf16x2 __attribute__((ext_vector_type(2)));
typedef __amdgpu_buffer_rsrc_t rsrc_t;
#define DI __device__ __forceinline__
#define SB0() __builtin_amdgcn_sched_barrier(0)
#define CBAR() asm volatile("" ::: "memory")
#define PIN8(f) asm volatile("" : "+v"(f[0]), "+v"(f[1]), "+v"(f[2]), "+v"(f[3]), "+v"(f[4]), "+v"(f[5]), "+v"(f[6]), "+v"(f[7]))
#define VMWAIT(n) asm volatile("s_waitcnt vmcnt(%0)" :: "n"(n) : "memory")

#define PREF 1
#define STAMPS 0
#define KWIN 3
#define VWIN 4
#define WEARLY 4

constexpr int WS_EPOCH = 0;
constexpr int WS_FLAGS = 1024, FLAG_REP = 2304;
constexpr int WS_MPA = 32768, MPA_SLOT = 4096 + 256;
constexpr int WS_H = WS_MPA + 256 * MPA_SLOT, HREP = 32768 + 256;
constexpr int WS_ARENA = 2 << 20;
static_assert(WS_H + 8 * HREP <= WS_ARENA, "map");
constexpr int WS_QP = WS_ARENA;
constexpr int WS_OP = WS_QP + (512 << 10);
constexpr int WS_XP = WS_OP + (512 << 10);
constexpr int WS_MPB = WS_XP + (512 << 10);
constexpr int WS_DP = WS_MPB + (512 << 10);
constexpr int REPX = 16384 + 2048 + 256;
constexpr int WS_X1 = WS_DP + (256 << 10);
constexpr int WS_GU = WS_X1 + 8 * REPX;
constexpr int WS_SB = WS_GU + (112 << 10);
constexpr int WS_ML = WS_SB + (32 << 10);
constexpr int WS_END = WS_ML + (16 << 10);
constexpr int ARENA_PER = 11 * 1024;
static_assert(WS_END <= WS_ARENA + 256 * ARENA_PER, "arena");
constexpr int WS_BYTES = 8 << 20;
constexpr unsigned SENT = 0xFFFFFFFFu;
#define CAPSH 0
constexpr int GCAP = 1 << (15 - CAPSH);
constexpr int LCAP = 1 << (22 - CAPSH);

enum { C_RESET = 0, C_GO, C_M1, C_M1S, C_R1, C_Q, C_QS, C_QG, C_MLW, C_MLF, C_M2, C_VI, C_O, C_T1, C_T2, C_PRE, C_C12, C_T3, C_T4, C_N = 32 };

DI float bflo(unsigned v) { return __builtin_bit_cast(float, v << 16); }
DI float bfhi(unsigned v) { return __builtin_bit_cast(float, v & 0xffff0000u); }
DI unsigned pk2(float a, float b) { bf16x2 v = {(__bf16)a, (__bf16)b}; unsigned r = __builtin_bit_cast(unsigned, v); asm("" : "+v"(r)); return r; }
DI float rbf(float a) { return (float)(__bf16)a; }
DI unsigned canon(unsigned u) { return u < 0xFFFFFFFEu ? u : 0xFFFFFFFEu; }
DI unsigned fbits(float f) { return __builtin_bit_cast(unsigned, f); }
DI float bitsf(unsigned u) { return __builtin_bit_cast(float, u); }
DI rsrc_t mk(const void* p, unsigned bytes) { return __builtin_amdgcn_make_buffer_rsrc((void*)p, 0, bytes, 0x00020000); }
DI v4u ld_nt(rsrc_t r, int v, int s) { return __builtin_amdgcn_raw_buffer_load_b128(r, v, s, 2); }
DI v4u ld_pl(rsrc_t r, int v, int s) { return __builtin_amdgcn_raw_buffer_load_b128(r, v, s, 0); }
DI unsigned ld_pl1(rsrc_t r, int v, int s) { return __builtin_amdgcn_raw_buffer_load_b32(r, v, s, 0); }
DI v4u ld_sc(rsrc_t r, int v, int s) { return __builtin_amdgcn_raw_buffer_load_b128(r, v, s, 16); }
DI v2u ld_sc2(rsrc_t r, int v, int s) { return __builtin_amdgcn_raw_buffer_load_b64(r, v, s, 16); }
DI unsigned ld_sc1(rsrc_t r, int v, int s) { return __builtin_amdgcn_raw_buffer_load_b32(r, v, s, 16); }
DI void st_sc(v4u x, rsrc_t r, int v, int s) { __builtin_amdgcn_raw_buffer_store_b128(x, r, v, s, 16); }
DI void st_sc2(v2u x, rsrc_t r, int v, int s) { __builtin_amdgcn_raw_buffer_store_b64(x, r, v, s, 16); }
DI void st_sc1(unsigned x, rsrc_t r, int v, int s) { __builtin_amdgcn_raw_buffer_store_b32(x, r, v, s, 16); }
DI f32x4 mfma(v4u a, v4u b, f32x4 c) {
  return __builtin_amdgcn_mfma_f32_16x16x32_bf16(__builtin_bit_cast(bf16x8, a), __builtin_bit_cast(bf16x8, b), c, 0, 0, 0);
}
template <int CTRL> DI unsigned dppu(unsigned x) { return __builtin_amdgcn_update_dpp(0u, x, CTRL, 0xF, 0xF, true); }
template <int CTRL> DI float dppf(float x) { return bitsf(dppu<CTRL>(fbits(x))); }
DI float add8(float x) { return x + dppf<0x128>(x); }
DI float add16(float x) { auto s = __builtin_amdgcn_permlane16_swap(fbits(x), fbits(x), false, false); return bitsf(s[0]) + bitsf(s[1]); }
DI float add32(float x) { auto s = __builtin_amdgcn_permlane32_swap(fbits(x), fbits(x), false, false); return bitsf(s[0]) + bitsf(s[1]); }
DI float max8(float x) { return fmaxf(x, dppf<0x128>(x)); }
DI float max16(float x) { auto s = __builtin_amdgcn_permlane16_swap(fbits(x), fbits(x), false, false); return fmaxf(bitsf(s[0]), bitsf(s[1])); }
DI float max32(float x) { auto s = __builtin_amdgcn_permlane32_swap(fbits(x), fbits(x), false, false); return fmaxf(bitsf(s[0]), bitsf(s[1])); }
DI float row_sum(float x) { x += dppf<0xB1>(x); x += dppf<0x4E>(x); x += dppf<0x141>(x); return x + dppf<0x128>(x); }
DI float sum32(float x) { return add16(row_sum(x)); }
DI float wave_sum(float x) { return add32(sum32(x)); }
DI float ex2(float x) { return __builtin_amdgcn_exp2f(x); }
DI float rcp(float x) { return __builtin_amdgcn_rcpf(x); }
constexpr float LOG2E = 1.4426950408889634f;
DI unsigned pair_pack(float v) { return dppu<0xA0>(pk2(v, dppf<0xB1>(v))); }

DI void lds_arrive(unsigned* c, int lane) {
  CBAR();
  if (lane == 0) __hip_atomic_fetch_add(c, 1u, __ATOMIC_RELAXED, __HIP_MEMORY_SCOPE_WORKGROUP);
  CBAR();
}
template <int SLEEP> DI void lds_wait(unsigned* c, unsigned target) {
#pragma unroll 1
  for (int i = 0; i < LCAP; ++i) {
    unsigned v = __hip_atomic_load(c, __ATOMIC_RELAXED, __HIP_MEMORY_SCOPE_WORKGROUP);
    if (__builtin_amdgcn_readfirstlane(v) >= target) break;
    if (SLEEP) __builtin_amdgcn_s_sleep(SLEEP);
    CBAR();
  }
  CBAR();
}
DI unsigned umax4(v4u x) { return max(max(x.x, x.y), max(x.z, x.w)); }
template <int N, int SLEEP = 1> DI void poll4(v4u (&v)[N], rsrc_t r, int voff, int sbase, int sstride) {
  CBAR();
#pragma unroll 1
  for (int i = 0; i < GCAP; ++i) {
#pragma unroll
    for (int n = 0; n < N; ++n) v[n] = ld_sc(r, voff, sbase + n * sstride);
    unsigned m = 0;
#pragma unroll
    for (int n = 0; n < N; ++n) m = max(m, umax4(v[n]));
    if (__builtin_amdgcn_ballot_w64(m == SENT) == 0) break;
    __builtin_amdgcn_s_sleep(SLEEP);
    CBAR();
  }
  CBAR();
}
template <int N, int SLEEP = 1> DI void poll1(unsigned (&v)[N], rsrc_t r, int voff, int sbase, int sstride) {
  CBAR();
#pragma unroll 1
  for (int i = 0; i < GCAP; ++i) {
#pragma unroll
    for (int n = 0; n < N; ++n) v[n] = ld_sc1(r, voff, sbase + n * sstride);
    unsigned m = 0;
#pragma unroll
    for (int n = 0; n < N; ++n) m = max(m, v[n]);
    if (__builtin_amdgcn_ballot_w64(m == SENT) == 0) break;
    __builtin_amdgcn_s_sleep(SLEEP);
    CBAR();
  }
  CBAR();
}
DI void pollt(v4u (&v)[4], rsrc_t r, int voff, int sbase, int sh, int sl, unsigned ta, unsigned tb) {
  CBAR();
#pragma unroll 1
  for (int i = 0; i < GCAP; ++i) {
#pragma unroll
    for (int n = 0; n < 4; ++n) v[n] = ld_sc(r, voff, sbase + (n >> 1) * sh + (n & 1) * sl);
    unsigned m = 0;
#pragma unroll
    for (int n = 0; n < 4; ++n) m |= (v[n].y ^ ta) | (v[n].w ^ tb);
    if (__builtin_amdgcn_ballot_w64(m != 0) == 0) break;
    __builtin_amdgcn_s_sleep(1);
    CBAR();
  }
  CBAR();
}
DI void tr8(const v4u (&w)[8], v4u (&f)[8]) {
#pragma unroll
  for (int m = 0; m < 4; ++m)
#pragma unroll
    for (int p = 0; p < 4; ++p) {
      f[2 * m][p] = __builtin_amdgcn_perm(w[2 * p + 1][m], w[2 * p][m], 0x05040100u);
      f[2 * m + 1][p] = __builtin_amdgcn_perm(w[2 * p + 1][m], w[2 * p][m], 0x07060302u);
    }
}
DI void kfold_store(const f32x4 (&d)[8], float* slot, int lane) {
  const bool lo = (lane & 8) == 0;
  float o[8];
#pragma unroll
  for (int j = 0; j < 8; ++j) {
    float x = lo ? d[j][0] : d[j][3], y = lo ? d[j][2] : d[j][1];
    o[j] = x + dppf<0x128>(y);
  }
  *(f32x4*)(slot + lane * 8) = f32x4{o[0], o[1], o[2], o[3]};
  *(f32x4*)(slot + lane * 8 + 4) = f32x4{o[4], o[5], o[6], o[7]};
}
DI float sum8(const float* p, int stride) {
  return ((p[0] + p[stride]) + (p[2 * stride] + p[3 * stride])) + ((p[4 * stride] + p[5 * stride]) + (p[6 * stride] + p[7 * stride]));
}
DI f32x2 sum8x2(const float* p, int stride) {
  auto L = [&](int i) { return *(const f32x2*)(p + i * stride); };
  return ((L(0) + L(1)) + (L(2) + L(3))) + ((L(4) + L(5)) + (L(6) + L(7)));
}
DI v4u canon4(f32x4 s) { return v4u{canon(fbits(s[0])), canon(fbits(s[1])), canon(fbits(s[2])), canon(fbits(s[3]))}; }
DI f32x4 asf(v4u x) { return __builtin_bit_cast(f32x4, x); }
DI v4u silu8(v4u c) {
  v4u r;
#pragma unroll
  for (int m = 0; m < 4; ++m) {
    float a = bflo(c[m]), b = bfhi(c[m]);
    a = a * rcp(1.f + ex2(-a * LOG2E)); b = b * rcp(1.f + ex2(-b * LOG2E));
    r[m] = pk2(a, b);
  }
  return r;
}

struct Args {
  const u16 *x, *kc, *vc, *wq, *wo, *wg, *wu, *wd, *c, *wmod;
  u16* out;
  char* ws;
};

#define STAMP(i) do { } while (0)

__global__ void __launch_bounds__(768, 3) mega(Args a) {
  __shared__ __attribute__((aligned(16))) unsigned cnt[C_N];
  __shared__ __attribute__((aligned(16))) unsigned qs[8 * 68];
  __shared__ __attribute__((aligned(16))) float redC[8 * 512];
  __shared__ __attribute__((aligned(16))) float mlw[8 * 16];
  __shared__ __attribute__((aligned(16))) float mlf[16];
  __shared__ __attribute__((aligned(16))) float redO[8 * 1024];
  __shared__ __attribute__((aligned(16))) float redT[8 * 1152];
  __shared__ __attribute__((aligned(16))) float redU[8 * 512];

  const int bid = blockIdx.x, lane = threadIdx.x & 63;
  const int w = __builtin_amdgcn_readfirstlane(threadIdx.x >> 6);
  const int xc = bid & 7, jx = bid >> 3;
  const int ab = xc, akvh = jx >> 3, as = jx & 7;
  const int grp = ab * 4 + akvh;
  const bool typeB = jx >= 16;
  const int jb = jx - 16;
  const int g = lane >> 4, n = lane & 15;
  const int arow = (n >> 1) * 2048 + (n & 1) * 64 + g * 16;
  const bool doT3 = jx < 14;
  const rsrc_t rws = mk(a.ws, WS_BYTES);
  if (threadIdx.x < C_N) cnt[threadIdx.x] = 0;

  if (w < 8) {
    if (w == 0) STAMP(0);
    {
      const v4u S = {SENT, SENT, SENT, SENT};
      st_sc(S, rws, lane * 16, WS_ARENA + bid * ARENA_PER + w * 1024);
      st_sc(S, rws, lane * 16, WS_ARENA + bid * ARENA_PER + (w < 3 ? 8 + w : w) * 1024);
    }
    const rsrc_t rk = mk(a.kc, 64u << 20), rv = mk(a.vc, 64u << 20);
    const int kvbase = (grp * 8 + as) * (256 << 10) + w * (32 << 10);
    const int koff = kvbase + ((lane >> 2) & 3) * 256 + g * 64 + (lane & 3) * 16;
    const int voff = kvbase + lane * 16;
    const bool l0 = lane & 1, l1 = lane & 2;
    const int ta = xc * 14 + jx;
    const int hp = 2 * xc + (jb >> 3), cg = jb & 7;
    const int td = xc * 14 + jb;
    const bool doT4 = typeB && jb < 14;
    v4u k[32];
    v4u wt[16];
    v4u v[32];
    f32x4 S[8];
    v4u qf[4];
    auto ktr = [&](int t) {
#pragma unroll
      for (int m = 0; m < 4; ++m) {
        unsigned r0 = k[4 * t][m], r1 = k[4 * t + 1][m], r2 = k[4 * t + 2][m], r3 = k[4 * t + 3][m];
        const unsigned d0 = dppu<0xB1>(r0), d1 = dppu<0xB1>(r1), d2 = dppu<0xB1>(r2), d3 = dppu<0xB1>(r3);
        const unsigned s0 = l0 ? d1 : r0, s1 = l0 ? r1 : d0, s2 = l0 ? d3 : r2, s3 = l0 ? r3 : d2;
        const unsigned e0 = dppu<0x4E>(s0), e1 = dppu<0x4E>(s1), e2 = dppu<0x4E>(s2), e3 = dppu<0x4E>(s3);
        k[4 * t][m] = l1 ? e2 : s0; k[4 * t + 2][m] = l1 ? s2 : e0;
        k[4 * t + 1][m] = l1 ? e3 : s1; k[4 * t + 3][m] = l1 ? s3 : e1;
      }
      asm volatile("" : "+v"(k[4 * t]), "+v"(k[4 * t + 1]), "+v"(k[4 * t + 2]), "+v"(k[4 * t + 3]));
      SB0();
    };
    auto vld = [&](int i) { SB0(); v[i] = ld_nt(rv, voff, i * 1024); SB0(); };
    SB0();
#pragma unroll
    for (int i = 0; i < 32; ++i) {
      k[i] = ld_nt(rk, koff, i * 1024);
      SB0();
      if (i == KWIN - 1) {
        asm volatile("s_waitcnt lgkmcnt(0)\n\ts_barrier\n\ts_waitcnt vmcnt(%0)" :: "n"(KWIN) : "memory");
        lds_arrive(&cnt[C_RESET], lane);
      }
      if (i >= KWIN - 1 && i < 31) VMWAIT(KWIN - 1);
      SB0();
      if (i >= KWIN + 2 && ((i - KWIN + 2) & 3) == 0 && i < 31) ktr((i - KWIN + 2) / 4 - 1);
    }
    constexpr int KDONE = (30 - KWIN + 2) / 4;
    if (w == 0) STAMP(1);
    lds_wait<2>(&cnt[C_QG], 4);
    if (w == 0) STAMP(2);
#pragma unroll
    for (int i = 0; i < 4; ++i) qf[i] = *(const v4u*)(qs + (n & 7) * 68 + g * 16 + i * 4);
#pragma unroll
    for (int u = 0; u < 8; ++u) {
      if (u >= KDONE) ktr(u);
      f32x4 s = {0.f, 0.f, 0.f, 0.f};
#pragma unroll
      for (int i = 0; i < 4; ++i) s = mfma(k[4 * u + i], qf[i], s);
      S[u] = s;
      if (u == 1) { vld(0); vld(1); vld(2); }
      if (u == 3) vld(3);
      if (u == 6) vld(4);
      SB0();
    }
    float mw = -INFINITY;
#pragma unroll
    for (int t = 0; t < 8; ++t) {
      unsigned p0 = pk2(S[t][0], S[t][1]), p1 = pk2(S[t][2], S[t][3]);
      const float sc = 0.08838834764831845f;
      p0 = pk2(bflo(p0) * sc, bfhi(p0) * sc); p1 = pk2(bflo(p1) * sc, bfhi(p1) * sc);
      S[t] = f32x4{bflo(p0), bfhi(p0), bflo(p1), bfhi(p1)};
      mw = fmaxf(mw, fmaxf(fmaxf(S[t][0], S[t][1]), fmaxf(S[t][2], S[t][3])));
      if (t == 0 || t == 3 || t == 6) vld(5 + t / 3);
    }
    mw = max32(max16(mw));
    float lw = 0.f;
    const float nm = -mw * LOG2E;
    v4u P[4];
#pragma unroll
    for (int t = 0; t < 8; ++t) {
      float e0 = ex2(fmaf(S[t][0], LOG2E, nm)), e1 = ex2(fmaf(S[t][1], LOG2E, nm)), e2 = ex2(fmaf(S[t][2], LOG2E, nm)), e3 = ex2(fmaf(S[t][3], LOG2E, nm));
      lw += (e0 + e1) + (e2 + e3);
      unsigned p0 = pk2(e0, e1), p1 = pk2(e2, e3);
      asm volatile("" : "+v"(p0), "+v"(p1));
      P[t >> 1][(t & 1) * 2] = p0; P[t >> 1][(t & 1) * 2 + 1] = p1;
      if (t == 1 || t == 4 || t == 7) vld(8 + t / 3);
    }
    lw = add32(add16(lw));
    {
      if (lane < 8) *(f32x2*)(mlw + w * 16 + lane * 2) = f32x2{mw, lw};
      CBAR();
      unsigned old = 0;
      if (lane == 0) old = __hip_atomic_fetch_add(&cnt[C_MLW], 1u, __ATOMIC_RELAXED, __HIP_MEMORY_SCOPE_WORKGROUP);
      old = __builtin_amdgcn_readfirstlane(old);
      CBAR();
      if (old == 7) {
        f32x2 r = *(const f32x2*)(mlw + lane * 2);
        float M = max32(max16(max8(r[0])));
        float L = add32(add16(add8(r[1] * ex2((r[0] - M) * LOG2E))));
        if (lane < 8) *(f32x2*)(mlf + lane * 2) = f32x2{M, L};
        lds_arrive(&cnt[C_MLF], lane);
      }
    }
    if (w == 0) STAMP(3);
    f32x4 O[8];
#pragma unroll
    for (int j = 0; j < 8; ++j) O[j] = f32x4{0.f, 0.f, 0.f, 0.f};
    v4u sbcb[4];
    const int auxv = typeB ? (n >> 1) * 64 : arow;
    const int auxb = typeB ? WS_ML + (2 * hp + (w >> 2)) * 512 : WS_SB + w * 256;
    const int auxh = typeB ? 32 : 16384, auxl = typeB ? 16 : 128;
    const rsrc_t rw0 = mk(typeB ? a.wo : (ta >= 56 ? a.wu : a.wg), typeB ? (8u << 20) : (doT3 ? (7u << 20) : 0u));
    const int wstride = typeB ? 2048 : 7168;
    const int woff = typeB ? ((2 * hp + (w >> 2)) * 128 + (w & 3) * 32 + g * 8) * 2048 + cg * 256 + n * 16
                           : (w * 128 + (n >> 3) * 32 + g * 8) * 7168 + (ta % 56) * 128 + (n & 7) * 16;
    auto pv = [&](int blk) {
#pragma unroll
      for (int m = 0; m < 4; ++m) {
        v4u f0, f1;
#pragma unroll
        for (int p = 0; p < 4; ++p) {
          f0[p] = __builtin_amdgcn_perm(v[8 * blk + 2 * p + 1][m], v[8 * blk + 2 * p][m], 0x05040100u);
          f1[p] = __builtin_amdgcn_perm(v[8 * blk + 2 * p + 1][m], v[8 * blk + 2 * p][m], 0x07060302u);
        }
        O[2 * m] = mfma(f0, P[blk], O[2 * m]);
        O[2 * m + 1] = mfma(f1, P[blk], O[2 * m + 1]);
        SB0();
      }
    };
    static_assert(VWIN >= 2 && VWIN <= 5, "");
#pragma unroll
    for (int i = 11; i < 32; ++i) {
      VMWAIT(VWIN - 1);
      vld(i);
      if (i == 12) pv(0);
      if (i == 15 + VWIN) pv(1);
      if (i == 23 + VWIN) pv(2);
    }
    lds_arrive(&cnt[C_VI], lane);
    float fs;
    {
      lds_wait<1>(&cnt[C_MLF], 1);
      f32x2 ml = *(const f32x2*)(mlf + (n & 7) * 2);
      fs = ex2((mw - ml[0]) * LOG2E) * rcp(ml[1]);
      asm volatile("" : "+v"(fs));
    }
    lds_wait<1>(&cnt[C_VI], 8);
    SB0();
#pragma unroll
    for (int i = 0; i < 4; ++i) sbcb[i] = ld_sc(rws, auxv, auxb + (i >> 1) * auxh + (i & 1) * auxl);
#pragma unroll
    for (int i = 0; i < WEARLY; ++i) wt[i] = ld_nt(rw0, woff, i * wstride);
    SB0();
    pv(3);
    if (w == 0) STAMP(4);
    if (w == 7) STAMP(26);
    {
      if (n < 8) {
        float* dst = redO + w * 1024 + n * 128 + g * 32;
#pragma unroll
        for (int r = 0; r < 4; ++r) {
          *(f32x4*)(dst + r * 8) = f32x4{O[0][r] * fs, O[1][r] * fs, O[2][r] * fs, O[3][r] * fs};
          *(f32x4*)(dst + r * 8 + 4) = f32x4{O[4][r] * fs, O[5][r] * fs, O[6][r] * fs, O[7][r] * fs};
        }
      }
      lds_arrive(&cnt[C_O], lane);
      lds_wait<0>(&cnt[C_O], 8);
      const f32x2 s = sum8x2(redO + w * 128 + lane * 2, 1024);
      st_sc1(canon(pk2(s[0], s[1])), rws, g * 4096 + n * 4, WS_OP + (akvh * 8 + w) * 16384 + (as >> 1) * 1024 + ab * 128 + (as & 1) * 64);
    }
    SB0();
#pragma unroll
    for (int i = WEARLY; i < 8; ++i) wt[i] = ld_nt(rw0, woff, i * wstride);
    if (doT3) {
#pragma unroll
      for (int i = 0; i < 8; ++i) wt[8 + i] = ld_nt(rw0, woff, (64 + i) * 7168);
    }
    SB0();
    if (w == 0) STAMP(5);
    CBAR();
#pragma unroll 1
    for (int i = 0; i < GCAP; ++i) {
      unsigned m = max(max(umax4(sbcb[0]), umax4(sbcb[1])), max(umax4(sbcb[2]), umax4(sbcb[3])));
      if (__builtin_amdgcn_ballot_w64(m == SENT) == 0) break;
      __builtin_amdgcn_s_sleep(1);
      CBAR();
#pragma unroll
      for (int j = 0; j < 4; ++j) sbcb[j] = ld_sc(rws, auxv, auxb + (j >> 1) * auxh + (j & 1) * auxl);
    }
    CBAR();
    v4u f[8];
    if (typeB) {
      float F[4];
      {
        float M = -INFINITY, tot = 0.f;
#pragma unroll
        for (int p = 0; p < 4; ++p) M = fmaxf(M, fmaxf(bitsf(sbcb[p][0]), bitsf(sbcb[p][2])));
#pragma unroll
        for (int p = 0; p < 4; ++p) {
          const float w0 = bitsf(sbcb[p][1]) * ex2((bitsf(sbcb[p][0]) - M) * LOG2E), w1 = bitsf(sbcb[p][3]) * ex2((bitsf(sbcb[p][2]) - M) * LOG2E);
          tot += w0 + w1;
          F[p] = l0 ? w1 : w0;
        }
        tot = rcp(tot);
#pragma unroll
        for (int p = 0; p < 4; ++p) F[p] *= tot;
      }
      asm volatile("" : "+v"(F[0]), "+v"(F[1]), "+v"(F[2]), "+v"(F[3]));
      tr8((const v4u(&)[8])wt[0], f);
      PIN8(f);
      if (w == 0) STAMP(27);
      v4u oa[4];
      poll4<4>(oa, rws, n * 64 + g * 16, WS_OP + (2 * hp + (w >> 2)) * 16384 + (w & 3) * 4096, 1024);
      if (w == 0) STAMP(6);
      v4u oo;
#pragma unroll
      for (int m = 0; m < 4; ++m) {
        float lo = bflo(oa[0][m]) * F[0], hi = bfhi(oa[0][m]) * F[0];
#pragma unroll
        for (int p = 1; p < 4; ++p) { lo = fmaf(bflo(oa[p][m]), F[p], lo); hi = fmaf(bfhi(oa[p][m]), F[p], hi); }
        oo[m] = pk2(lo + dppf<0xB1>(lo), hi + dppf<0xB1>(hi));
      }
      f32x4 d[8];
      float* dst = redT + w * 1152 + g * 288 + n;
#pragma unroll
      for (int j = 0; j < 8; ++j) d[j] = mfma(oo, f[j], f32x4{0.f, 0.f, 0.f, 0.f});
#pragma unroll
      for (int j = 0; j < 8; ++j) { dst[j * 16] = d[j][0]; dst[144 + j * 16] = d[j][2]; }
      lds_arrive(&cnt[C_T1], lane);
      lds_wait<0>(&cnt[C_T1], 8);
      {
        const f32x2 s = sum8x2(redT + w * 144 + ((lane >> 1) & 7) * 16 + g * 4 + (lane & 1) * 2, 1152);
        st_sc2(v2u{canon(fbits(s[0])), canon(fbits(s[1]))}, rws, g * 2048 + n * 8, WS_XP + (w * 32 + cg * 4) * 2048 + hp * 128);
      }
      if (w == 0) STAMP(7);
      if (doT4) {
        const int ks = td >> 4, c16 = td & 15;
        lds_wait<1>(&cnt[C_T2], 2);
        const rsrc_t rw = mk(a.wd, 7u << 20);
        const int off = (ks * 512 + w * 64 + (n >> 3) * 32 + g * 8) * 2048 + c16 * 128 + (n & 7) * 16;
#pragma unroll
        for (int i = 0; i < 8; ++i) wt[i] = ld_nt(rw, off, i * 2048);
        tr8((const v4u(&)[8])wt[0], f);
        PIN8(f);
        if (w == 0) STAMP(12);
        v4u gu[2];
        poll4<2>(gu, rws, n * 64 + g * 16, WS_GU + (ks * 8 + w) * 1024, 56 * 1024);
        if (w == 0) STAMP(13);
        v4u hh;
#pragma unroll
        for (int m = 0; m < 4; ++m) hh[m] = pk2(bflo(gu[0][m]) * bflo(gu[1][m]), bfhi(gu[0][m]) * bfhi(gu[1][m]));
#pragma unroll
        for (int j = 0; j < 8; ++j) d[j] = mfma(hh, f[j], f32x4{0.f, 0.f, 0.f, 0.f});
        kfold_store(d, redU + w * 512, lane);
        lds_arrive(&cnt[C_T4], lane);
        lds_wait<0>(&cnt[C_T4], 8);
        const float t = sum8(redU + w * 64 + lane, 512);
        st_sc1(canon(fbits(t)), rws, (lane >> 5) * 1024 + (lane & 31) * 4, WS_DP + (w * 32 + c16 * 2) * 1024 + ks * 128);
        if (w == 0) STAMP(14);
      }
    } else if (doT3) {
      v4u f1[8];
      tr8((const v4u(&)[8])wt[0], f);
      PIN8(f);
      SB0();
      tr8((const v4u(&)[8])wt[8], f1);
      PIN8(f1);
      SB0();
      f32x4 d[8];
#pragma unroll
      for (int j = 0; j < 8; ++j) d[j] = mfma(sbcb[1], f1[j], mfma(sbcb[0], f[j], f32x4{0.f, 0.f, 0.f, 0.f}));
      kfold_store(d, redT + w * 512, lane);
#pragma unroll
      for (int j = 0; j < 8; ++j) d[j] = mfma(sbcb[3], f1[j], mfma(sbcb[2], f[j], f32x4{0.f, 0.f, 0.f, 0.f}));
      kfold_store(d, redT + 4096 + w * 512, lane);
      lds_arrive(&cnt[C_C12], lane);
      lds_wait<1>(&cnt[C_C12], 8);
      float c2 = sum8(redT + w * 64 + lane, 512), c1 = sum8(redT + 4096 + w * 64 + lane, 512);
      asm volatile("" : "+v"(c1), "+v"(c2));
      if (w == 0) STAMP(9);
      lds_wait<1>(&cnt[C_PRE], 1);
      v4u xr[2];
      v2u st;
      {
        const int sb0 = WS_X1 + xc * REPX;
        CBAR();
#pragma unroll 1
        for (int i = 0; i < GCAP; ++i) {
          xr[0] = ld_sc(rws, arow, sb0 + w * 256); xr[1] = ld_sc(rws, arow, sb0 + w * 256 + 128);
          st = ld_sc2(rws, (lane & 31) * 8, sb0 + 16384 + w * 256);
          unsigned m = max(max(umax4(xr[0]), umax4(xr[1])), max(st.x, st.y));
          if (__builtin_amdgcn_ballot_w64(m == SENT) == 0) break;
          __builtin_amdgcn_s_sleep(1);
          CBAR();
        }
        CBAR();
      }
      if (w == 0) STAMP(10);
#pragma unroll
      for (int j = 0; j < 8; ++j) d[j] = mfma(xr[1], f1[j], mfma(xr[0], f[j], f32x4{0.f, 0.f, 0.f, 0.f}));
      float mean, rden;
      {
        const float mb = bitsf(st.x);
        const float mu = sum32(mb) * (1.f / 32.f);
        const float var = sum32(fmaf((mb - mu) * 32.f, mb - mu, bitsf(st.y))) * (1.f / 1024.f);
        mean = rbf(mu);
        rden = rcp(rbf(__builtin_amdgcn_sqrtf(rbf(rbf(var) + 1e-5f))));
      }
      kfold_store(d, redU + w * 512, lane);
      lds_arrive(&cnt[C_T3], lane);
      c2 = fmaf(-mean * rden, c1, c2);
      lds_wait<0>(&cnt[C_T3], 8);
      float t = fmaf(sum8(redU + w * 64 + lane, 512), rden, c2);
      if (ta < 56) { t = rbf(t); t = t > 0.f ? t : t * 0.f; }
      const unsigned u = canon(pk2(t, dppf<0xB1>(t)));
      if (!l0) st_sc1(u, rws, lane * 2, WS_GU + ta * 1024 + w * 128);
      if (w == 0) STAMP(11);
    }
  } else {
    const int cw = w - 8;
    __builtin_amdgcn_s_setprio(2);
    const u64 ep = *(const __attribute__((address_space(4))) u64*)(uintptr_t)a.ws;
    const u64 tag = ep + 0x9E3779B97F4A7C15ull;
    unsigned tlo = (unsigned)tag, thi = (unsigned)(tag >> 32);
    asm volatile("" : "+s"(tlo), "+s"(thi));
    const rsrc_t rc = mk(a.c, 16384), rm = mk(a.wmod, 12u << 20), rx = mk(a.x, 16384);
    const int wlane_r = (n >> 3) * 32 + g * 8, wlane_c = (n & 7) * 16;
    const int rcb = 2 * jb + cw, rr = xc * 32 + rcb, col = lane & 31, half = lane >> 5;
    unsigned xo = 0;
    if (cw >= 2) {
      const int h = cw - 2;
      const int ct = bid & 31, ks = bid >> 5;
      const int qct = bid & 63, qks = bid >> 6;
      v4u wm[8], wq0[8], wq1[8];
      const v4u cv = ld_pl(rc, arow, (ks * 128 + h * 64) * 2);
      {
        const int off = (ks * 128 + h * 64 + wlane_r) * 12288 + ct * 128 + wlane_c;
#pragma unroll
        for (int i = 0; i < 8; ++i) wm[i] = ld_nt(rm, off, i * 12288);
      }
      SB0();
      asm volatile("s_waitcnt lgkmcnt(0)\n\ts_barrier" ::: "memory");
      const v4u ca = silu8(cv);
      v4u f[8];
      f32x4 d[8];
      tr8(wm, f);
#pragma unroll
      for (int j = 0; j < 8; ++j) d[j] = mfma(ca, f[j], f32x4{0.f, 0.f, 0.f, 0.f});
      kfold_store(d, redC + h * 512, lane);
      lds_arrive(&cnt[C_M1], lane);
      lds_wait<0>(&cnt[C_M1], 2);
      {
        const float* p = redC + h * 256 + lane * 2;
        const f32x2 s0 = *(const f32x2*)p + *(const f32x2*)(p + 512), s1 = *(const f32x2*)(p + 128) + *(const f32x2*)(p + 640);
        st_sc(v4u{fbits(s0[0]), tlo, fbits(s0[1]), thi}, rws, lane * 16, WS_MPA + (ct * 8 + ks) * MPA_SLOT + h * 2048);
        st_sc(v4u{fbits(s1[0]), tlo, fbits(s1[1]), thi}, rws, lane * 16, WS_MPA + (ct * 8 + ks) * MPA_SLOT + h * 2048 + 1024);
      }
      lds_arrive(&cnt[C_M1S], lane);
      if (cw == 2) STAMP(16);
      SB0();
      {
        const rsrc_t rq = mk(a.wq, 8u << 20);
        const int off = (qks * 256 + h * 128 + wlane_r) * 8192 + qct * 128 + wlane_c;
#pragma unroll
        for (int i = 0; i < 16; ++i) {
          if (i >= 3) VMWAIT(2);
          SB0();
          if (i < 8) wq0[i] = ld_nt(rq, off, i * 8192); else wq1[i - 8] = ld_nt(rq, off, (56 + i) * 8192);
          SB0();
        }
      }
      v4u f2[8];
      tr8(wq0, f);
      tr8(wq1, f2);
      PIN8(f);
      PIN8(f2);
      if (cw == 2) STAMP(28);
      lds_wait<1>(&cnt[C_R1], 1);
      v4u ha[4];
      pollt(ha, rws, (n >> 1) * 4096 + (n & 1) * 128 + g * 32, WS_H + xc * HREP + (qks * 256 + h * 128) * 4, 256, 16, tlo, thi);
      if (cw == 2) STAMP(19);
      const v4u a0 = {ha[0].x, ha[0].z, ha[1].x, ha[1].z}, a1 = {ha[2].x, ha[2].z, ha[3].x, ha[3].z};
#pragma unroll
      for (int j = 0; j < 8; ++j) d[j] = mfma(a1, f2[j], mfma(a0, f[j], f32x4{0.f, 0.f, 0.f, 0.f}));
      kfold_store(d, redC + 1024 + h * 512, lane);
      lds_arrive(&cnt[C_Q], lane);
      lds_wait<0>(&cnt[C_Q], 2);
      {
        f32x4 s = *(const f32x4*)(redC + 1024 + h * 256 + lane * 4) + *(const f32x4*)(redC + 1536 + h * 256 + lane * 4);
        lds_wait<1>(&cnt[C_GO], 1);
        st_sc(canon4(s), rws, g * 65536 + n * 16, WS_QP + h * 4 * 65536 + (qct >> 4) * 16384 + qks * 4096 + (qct & 15) * 256);
      }
      lds_arrive(&cnt[C_QS], lane);
      if (cw == 2) STAMP(20);
    } else {
      v4u xr[2];
      unsigned xp = 0;
      if (PREF && cw == 0) {
        u64 pc;
        unsigned len;
        asm volatile("s_getpc_b64 %0\n.Lref%=:\n\ts_mov_b32 %1, .Lref%=-_Z4mega4Args" : "=s"(pc), "=s"(len));
        pc -= len;
        asm volatile("s_mov_b32 %0, _Z8mega_endv-_Z4mega4Args" : "=s"(len));
        const rsrc_t rcode = mk((const void*)pc, len);
        xr[0] = ld_pl(rcode, lane * 16, jx * 1024); xr[1] = ld_pl(rcode, lane * 16, jx * 1024 + 32768);
      }
      if (typeB) xo = ld_pl1(rx, (col >> 1) * 4, xc * 2048 + rcb * 64);
      if (cw == 1) {
        xr[0] = ld_pl(rx, lane * 16, xc * 2048); xr[1] = ld_pl(rx, lane * 16, xc * 2048 + 1024);
        xp = ld_pl1(rx, n * 4, xc * 2048 + jx * 64);
      }
      asm volatile("s_waitcnt lgkmcnt(0)\n\ts_barrier" ::: "memory");
      if (cw == 0) {
        lds_wait<1>(&cnt[C_RESET], 8);
        if (PREF) asm volatile("" :: "v"(xr[0]), "v"(xr[1]));
        if (lane < 8) st_sc2(v2u{tlo, thi}, rws, lane * FLAG_REP, WS_FLAGS + bid * 8);
        STAMP(15);
        CBAR();
#pragma unroll 1
        for (int i = 0; i < GCAP; ++i) {
          __builtin_amdgcn_s_sleep(12);
          v4u f0 = ld_sc(rws, lane * 16, WS_FLAGS + xc * FLAG_REP), f1 = ld_sc(rws, lane * 16, WS_FLAGS + xc * FLAG_REP + 1024);
          const unsigned bad = (f0.x ^ tlo) | (f0.y ^ thi) | (f0.z ^ tlo) | (f0.w ^ thi) | (f1.x ^ tlo) | (f1.y ^ thi) | (f1.z ^ tlo) | (f1.w ^ thi);
          if (__builtin_amdgcn_ballot_w64(bad != 0) == 0) break;
          CBAR();
        }
        lds_arrive(&cnt[C_GO], lane);
        if (bid == 0 && lane == 0) st_sc2(v2u{tlo, thi}, rws, 0, WS_EPOCH);
        STAMP(17);
      } else {
        float ln0, ln1;
        {
          const float x0 = bitsf(__builtin_amdgcn_readfirstlane(xr[0][0] << 16));
          float s = 0.f, q = 0.f;
#pragma unroll
          for (int i = 0; i < 8; ++i) {
            const float d0 = bflo(xr[i >> 2][i & 3]) - x0, d1 = bfhi(xr[i >> 2][i & 3]) - x0;
            s += d0 + d1; q = fmaf(d0, d0, fmaf(d1, d1, q));
          }
          s = wave_sum(s); q = wave_sum(q);
          const float md = s * (1.f / 1024.f);
          const float var = fmaxf(q * (1.f / 1024.f) - md * md, 0.f);
          const float mean = rbf(x0 + md);
          const float den = rbf(__builtin_sqrtf(rbf(rbf(var) + 1e-5f)));
          ln0 = rbf(rbf(bflo(xp) - mean) / den); ln1 = rbf(rbf(bfhi(xp) - mean) / den);
          asm volatile("" : "+v"(ln0), "+v"(ln1));
        }
        lds_wait<1>(&cnt[C_M1S], 2);
        v4u mp[4];
        pollt(mp, rws, g * MPA_SLOT + n * 16, WS_MPA + (jx >> 1) * 8 * MPA_SLOT + xc * 512 + (jx & 1) * 256, 128 * MPA_SLOT, 4 * MPA_SLOT, tlo, thi);
        const float sa0 = rbf(add32(add16(bitsf(mp[0].x) + bitsf(mp[1].x)))), sa1 = rbf(add32(add16(bitsf(mp[0].z) + bitsf(mp[1].z))));
        const float ca0 = rbf(add32(add16(bitsf(mp[2].x) + bitsf(mp[3].x)))), ca1 = rbf(add32(add16(bitsf(mp[2].z) + bitsf(mp[3].z))));
        const unsigned u = pk2(rbf(ln0 * rbf(1.f + ca0)) + sa0, rbf(ln1 * rbf(1.f + ca1)) + sa1);
        st_sc(v4u{dppu<0xA0>(u), tlo, dppu<0xF5>(u), thi}, rws, (g * 2 + (n & 1)) * HREP + (n >> 1) * 16, WS_H + xc * 4096 + jx * 128);
        lds_arrive(&cnt[C_R1], lane);
        STAMP(18);
      }
    }
    lds_wait<1>(&cnt[C_QS], 2);
    {
      v4u qp[4];
      poll4<4>(qp, rws, lane * 16, WS_QP + grp * 16384 + cw * 1024, 4096);
      f32x4 s = (asf(qp[0]) + asf(qp[1])) + (asf(qp[2]) + asf(qp[3]));
      *(v2u*)(qs + (cw * 2 + (lane >> 5)) * 68 + (lane & 31) * 2) = v2u{pk2(s[0], s[1]), pk2(s[2], s[3])};
      lds_arrive(&cnt[C_QG], lane);
      if (cw == 0) STAMP(21);
    }
    __builtin_amdgcn_s_setprio(0);
    lds_wait<2>(&cnt[C_QG], 4);
    {
      const int ct = bid & 63, ks = bid >> 6;
      v4u w0[8];
      const int r0 = ks * 256 + cw * 64;
      const v4u c0 = ld_pl(rc, arow, r0 * 2);
      const int off = (r0 + wlane_r) * 12288 + 4096 + ct * 128 + wlane_c;
#pragma unroll
      for (int i = 0; i < 8; ++i) w0[i] = ld_nt(rm, off, i * 12288);
      SB0();
      const v4u a0 = silu8(c0);
      v4u f[8];
      tr8(w0, f);
      f32x4 d[8];
#pragma unroll
      for (int j = 0; j < 8; ++j) d[j] = mfma(a0, f[j], f32x4{0.f, 0.f, 0.f, 0.f});
      kfold_store(d, redC + 2048 + cw * 512, lane);
      lds_arrive(&cnt[C_M2], lane);
      lds_wait<1>(&cnt[C_M2], 4);
      const float* src = redC + 2048 + (lane >> 5) * 1024 + cw * 128 + (lane & 31) * 4;
      f32x4 s = *(const f32x4*)src + *(const f32x4*)(src + 512);
      s = f32x4{add32(s[0]), add32(s[1]), add32(s[2]), add32(s[3])};
      if (lane < 32) st_sc(canon4(s), rws, g * 65536 + (n >> 3) * 2048 + (n & 7) * 16, WS_MPB + cw * 131072 + (ct & 15) * 4096 + (ct >> 4) * 512 + ks * 128);
      if (cw == 0) STAMP(22);
    }
    if (cw == 3) {
      lds_wait<8>(&cnt[C_MLF], 1);
      if (lane < 8) {
        const f32x2 ml = *(const f32x2*)(mlf + lane * 2);
        st_sc2(v2u{canon(fbits(ml[0])), canon(fbits(ml[1]))}, rws, lane * 512, WS_ML + akvh * 4096 + ab * 64 + as * 8);
      }
      STAMP(23);
    }
    __builtin_amdgcn_s_setprio(2);
    if (doT3 && cw == 0) {
      lds_wait<8>(&cnt[C_O], 8);
      v4u t[2];
      poll4<2, 4>(t, rws, lane * 16, WS_XP + (bid * 2 + 1) * 2048, 1024);
      lds_arrive(&cnt[C_PRE], lane);
      STAMP(29);
    }
    if (typeB && cw < 2) {
      float ga, gb, cb1;
      {
        unsigned mp[8];
        CBAR();
#pragma unroll 1
        for (int i = 0; i < GCAP; ++i) {
          unsigned m = 0;
#pragma unroll
          for (int j = 0; j < 8; ++j) { mp[j] = ld_sc1(rws, half * 256 + col * 4, WS_MPB + rr * 2048 + (j >> 1) * 512 + (j & 1) * 128); m = max(m, mp[j]); }
          if (__builtin_amdgcn_ballot_w64(m == SENT) == 0) break;
          __builtin_amdgcn_s_sleep(16);
          CBAR();
        }
        CBAR();
        ga = rbf(add32(bitsf(mp[0]) + bitsf(mp[1])));
        const float sb = add32(bitsf(mp[2]) + bitsf(mp[3]));
        cb1 = rbf(1.f + rbf(add32(bitsf(mp[4]) + bitsf(mp[5]))));
        gb = rbf(add32(bitsf(mp[6]) + bitsf(mp[7])));
        const float z = half ? cb1 : sb;
        const unsigned u = canon(pk2(z, dppf<0xB1>(z)));
        if (!(lane & 1)) st_sc1(u, rws, half * 16384 + col * 2, WS_SB + xc * 2048 + rcb * 64);
        if (cw == 0) STAMP(24);
      }
      const float xv = (lane & 1) ? bfhi(xo) : bflo(xo);
      lds_wait<4>(&cnt[C_T1], 8);
      float x1;
      {
        unsigned xp[8];
        poll1<8>(xp, rws, half * 1024 + (col & 7) * 16 + (col >> 3) * 4, WS_XP + rr * 2048, 128);
        const float s = add32(((bitsf(xp[0]) + bitsf(xp[1])) + (bitsf(xp[2]) + bitsf(xp[3]))) + ((bitsf(xp[4]) + bitsf(xp[5])) + (bitsf(xp[6]) + bitsf(xp[7]))));
        x1 = rbf(xv + rbf(ga * rbf(s)));
        const unsigned u = canon(pair_pack(x1 * cb1));
        const int vo = ((lane & 1) + 2 * half) * REPX + (col >> 1) * 4;
        st_sc1(u, rws, vo, WS_X1 + xc * 2048 + rcb * 64);
        st_sc1(u, rws, vo, WS_X1 + 4 * REPX + xc * 2048 + rcb * 64);
        const float x0 = bitsf(__builtin_amdgcn_readfirstlane(fbits(x1)));
        const float e = x1 - x0;
        const float sm = sum32(e), sq = sum32(e * e);
        const float md = sm * (1.f / 32.f);
        if (lane < 8) st_sc2(v2u{canon(fbits(x0 + md)), canon(fbits(fmaxf(sq - sm * md, 0.f)))}, rws, lane * REPX, WS_X1 + 16384 + xc * 256 + rcb * 8);
        lds_arrive(&cnt[C_T2], lane);
        if (cw == 0) STAMP(8);
      }
      if (jb < 14) lds_wait<1>(&cnt[C_T4], 8);
      {
        unsigned dp[4];
        CBAR();
#pragma unroll 1
        for (int i = 0; i < GCAP; ++i) {
#pragma unroll
          for (int j = 0; j < 3; ++j) dp[j] = ld_sc1(rws, half * 512 + col * 4, WS_DP + rr * 1024 + j * 128);
          dp[3] = ld_sc1(rws, half * 384 + col * 4, WS_DP + rr * 1024 + 384);
          const unsigned m = max(max(dp[0], dp[1]), max(dp[2], dp[3]));
          if (__builtin_amdgcn_ballot_w64(m == SENT) == 0) break;
          __builtin_amdgcn_s_sleep(1);
          CBAR();
        }
        CBAR();
        const float s = add32((bitsf(dp[0]) + bitsf(dp[1])) + (bitsf(dp[2]) + (half ? 0.f : bitsf(dp[3]))));
        const float o = x1 + rbf(gb * rbf(s));
        const unsigned u = pk2(o, dppf<0xB1>(o));
        if (lane < 32 && !(lane & 1)) *(unsigned*)(a.out + xc * 1024 + rcb * 32 + col) = u;
        if (cw == 0) STAMP(25);
      }
    }
  }
}

__global__ void mega_end() {}

extern "C" void kernel_launch(void* const* d_in, const int* in_sizes, int n_in, void* d_out, int out_size, void* d_ws, size_t ws_size, hipStream_t stream) {
  Args a;
  a.x = (const u16*)d_in[0]; a.kc = (const u16*)d_in[1]; a.vc = (const u16*)d_in[2]; a.wq = (const u16*)d_in[3]; a.wo = (const u16*)d_in[4];
  a.wg = (const u16*)d_in[6]; a.wu = (const u16*)d_in[7]; a.wd = (const u16*)d_in[8]; a.c = (const u16*)d_in[9]; a.wmod = (const u16*)d_in[10];
  a.out = (u16*)d_out; a.ws = (char*)d_ws;
  mega<<<256, 768, 0, stream>>>(a);
}
```

```cpp
#include <hip/hip_runtime.h>
#include <stdint.h>

typedef unsigned short u16;
typedef unsigned long long u64;
typedef unsigned v4u __attribute__((ext_vector_type(4)));
typedef unsigned v2u __attribute__((ext_vector_type(2)));
typedef float f32x4 __attribute__((ext_vector_type(4)));
typedef float f32x2 __attribute__((ext_vector_type(2)));
typedef __bf16 bf16x8 __attribute__((ext_vector_type(8)));
typedef __bf16 bf16x2 __attribute__((ext_vector_type(2)));
typedef __amdgpu_buffer_rsrc_t rsrc_t;
#define DI __device__ __forceinline__
#define SB0() __builtin_amdgcn_sched_barrier(0)
#define CBAR() asm volatile("" ::: "memory")
#define PIN8(f) asm volatile("" : "+v"(f[0]), "+v"(f[1]), "+v"(f[2]), "+v"(f[3]), "+v"(f[4]), "+v"(f[5]), "+v"(f[6]), "+v"(f[7]))
#define VMWAIT(n) asm volatile("s_waitcnt vmcnt(%0)" :: "n"(n) : "memory")

#define PREF 1
#define WDG 4
#define PRS 4
#define PRD 8
#define PRD4 16
#define STD 4
#define STS 2
#define WDW 2
#define STAMPS 0
#define NP 12
#define NS (NP + 64)
#define QSL 46
#define WIN 8
#define W1 4
#define S1 QSL
#define TAU 470
#define PA 150
#define P0 1
#define WE 2
#define WGW 8
#define ABL 0
#define LAY 1
#define DIAG 0
#define AMP 2

constexpr int WS_EPOCH = 0;
constexpr int WS_FLAGS = 1024, FLAG_REP = 2304;
constexpr int WS_MPA = 32768, MPA_SLOT = 4096 + 256;
constexpr int WS_H = WS_MPA + 256 * MPA_SLOT, HREP = 32768 + 256;
constexpr int WS_ARENA = 2 << 20;
static_assert(WS_H + 8 * HREP <= WS_ARENA, "map");
constexpr int WS_QP = WS_ARENA;
constexpr int WS_OP = WS_QP + (512 << 10);
constexpr int WS_XP = WS_OP + (512 << 10);
constexpr int WS_MPB = WS_XP + (512 << 10);
constexpr int WS_DP = WS_MPB + (512 << 10);
constexpr int REPX = 16384 + 1024 + 256;
constexpr int WS_X1 = WS_DP + (256 << 10);
constexpr int WS_GU = WS_X1 + 8 * REPX;
constexpr int WS_SB = WS_GU + (112 << 10);
constexpr int WS_ML = WS_SB + (32 << 10);
constexpr int WS_END = WS_ML + (16 << 10);
constexpr int ARENA_PER = 11 * 1024;
static_assert(WS_END <= WS_ARENA + 256 * ARENA_PER, "arena");
constexpr int WS_BYTES = 8 << 20;
constexpr unsigned SENT = 0xFFFFFFFFu;
#define CAPSH 0
constexpr int GCAP = 1 << (15 - CAPSH);
constexpr int LCAP = 1 << (22 - CAPSH);

enum { C_RESET = 0, C_GO, C_M1, C_M1S, C_R1, C_Q, C_QS, C_QG, C_MLW, C_MLF, C_M2, C_VI, C_O, C_T1, C_T2, C_PRE, C_C12, C_T3, C_T4, C_PARK, C_PKD, C_N = 32 };

DI float bflo(unsigned v) { return __builtin_bit_cast(float, v << 16); }
DI float bfhi(unsigned v) { return __builtin_bit_cast(float, v & 0xffff0000u); }
DI unsigned pk2(float a, float b) { bf16x2 v = {(__bf16)a, (__bf16)b}; unsigned r = __builtin_bit_cast(unsigned, v); asm("" : "+v"(r)); return r; }
DI float rbf(float a) { return (float)(__bf16)a; }
DI unsigned canon(unsigned u) { return u < 0xFFFFFFFEu ? u : 0xFFFFFFFEu; }
DI unsigned fbits(float f) { return __builtin_bit_cast(unsigned, f); }
DI float bitsf(unsigned u) { return __builtin_bit_cast(float, u); }
DI rsrc_t mk(const void* p, unsigned bytes) { return __builtin_amdgcn_make_buffer_rsrc((void*)p, 0, bytes, 0x00020000); }
DI v4u ld_nt(rsrc_t r, int v, int s) { return __builtin_amdgcn_raw_buffer_load_b128(r, v, s, 2); }
DI v4u ld_pl(rsrc_t r, int v, int s) { return __builtin_amdgcn_raw_buffer_load_b128(r, v, s, 0); }
DI unsigned ld_pl1(rsrc_t r, int v, int s) { return __builtin_amdgcn_raw_buffer_load_b32(r, v, s, 0); }
DI v4u ld_sc(rsrc_t r, int v, int s) { return __builtin_amdgcn_raw_buffer_load_b128(r, v, s, 16); }
DI v2u ld_sc2(rsrc_t r, int v, int s) { return __builtin_amdgcn_raw_buffer_load_b64(r, v, s, 16); }
DI unsigned ld_sc1(rsrc_t r, int v, int s) { return __builtin_amdgcn_raw_buffer_load_b32(r, v, s, 16); }
DI void st_sc(v4u x, rsrc_t r, int v, int s) { __builtin_amdgcn_raw_buffer_store_b128(x, r, v, s, 16); }
DI void st_sc2(v2u x, rsrc_t r, int v, int s) { __builtin_amdgcn_raw_buffer_store_b64(x, r, v, s, 16); }
DI void st_sc1(unsigned x, rsrc_t r, int v, int s) { __builtin_amdgcn_raw_buffer_store_b32(x, r, v, s, 16); }
DI f32x4 mfma(v4u a, v4u b, f32x4 c) {
  return __builtin_amdgcn_mfma_f32_16x16x32_bf16(__builtin_bit_cast(bf16x8, a), __builtin_bit_cast(bf16x8, b), c, 0, 0, 0);
}
template <int CTRL> DI unsigned dppu(unsigned x) { return __builtin_amdgcn_update_dpp(0u, x, CTRL, 0xF, 0xF, true); }
template <int CTRL> DI float dppf(float x) { return bitsf(dppu<CTRL>(fbits(x))); }
DI float add8(float x) { return x + dppf<0x128>(x); }
DI float add16(float x) { auto s = __builtin_amdgcn_permlane16_swap(fbits(x), fbits(x), false, false); return bitsf(s[0]) + bitsf(s[1]); }
DI float add32(float x) { auto s = __builtin_amdgcn_permlane32_swap(fbits(x), fbits(x), false, false); return bitsf(s[0]) + bitsf(s[1]); }
DI float max8(float x) { return fmaxf(x, dppf<0x128>(x)); }
DI float max16(float x) { auto s = __builtin_amdgcn_permlane16_swap(fbits(x), fbits(x), false, false); return fmaxf(bitsf(s[0]), bitsf(s[1])); }
DI float max32(float x) { auto s = __builtin_amdgcn_permlane32_swap(fbits(x), fbits(x), false, false); return fmaxf(bitsf(s[0]), bitsf(s[1])); }
DI float row_sum(float x) { x += dppf<0xB1>(x); x += dppf<0x4E>(x); x += dppf<0x141>(x); return x + dppf<0x128>(x); }
DI float sum32(float x) { return add16(row_sum(x)); }
DI float wave_sum(float x) { return add32(sum32(x)); }
DI float ex2(float x) { return __builtin_amdgcn_exp2f(x); }
DI float rcp(float x) { return __builtin_amdgcn_rcpf(x); }
constexpr float LOG2E = 1.4426950408889634f;
DI unsigned pair_pack(float v) { return dppu<0xA0>(pk2(v, dppf<0xB1>(v))); }

DI void lds_arrive(unsigned* c, int lane) {
  CBAR();
  if (lane == 0) __hip_atomic_fetch_add(c, 1u, __ATOMIC_RELAXED, __HIP_MEMORY_SCOPE_WORKGROUP);
  CBAR();
}
template <int SLEEP> DI void lds_wait(unsigned* c, unsigned target) {
#pragma unroll 1
  for (int i = 0; i < LCAP; ++i) {
    unsigned v = __hip_atomic_load(c, __ATOMIC_RELAXED, __HIP_MEMORY_SCOPE_WORKGROUP);
    if (__builtin_amdgcn_readfirstlane(v) >= target) break;
    if (SLEEP) __builtin_amdgcn_s_sleep(SLEEP);
    CBAR();
  }
  CBAR();
}
DI unsigned umax4(v4u x) { return max(max(x.x, x.y), max(x.z, x.w)); }
template <int N, class T, class F> DI void gat_issue(T (&v)[N], F f) {
  CBAR();
#pragma unroll
  for (int n = 0; n < N; ++n) v[n] = f(n);
  CBAR();
}
template <int SLEEP, int N, class T, class F, class M> DI void gat_finish(T (&v)[N], F f, M miss) {
  CBAR();
  bool m[N], any = false;
#pragma unroll
  for (int n = 0; n < N; ++n) { m[n] = miss(v[n]); any |= m[n]; }
  if (__builtin_amdgcn_ballot_w64(any) != 0) {
#pragma unroll 1
    for (int i = 0; i < GCAP; ++i) {
      if (SLEEP) __builtin_amdgcn_s_sleep(SLEEP);
      CBAR();
#pragma unroll
      for (int n = 0; n < N; ++n) if (m[n]) v[n] = f(n);
      CBAR();
      any = false;
#pragma unroll
      for (int n = 0; n < N; ++n) { m[n] = miss(v[n]); any |= m[n]; }
      if (__builtin_amdgcn_ballot_w64(any) == 0) break;
    }
  }
  CBAR();
}
template <int SLEEP, int N, class T, class F, class M> DI void gather(T (&v)[N], F f, M miss) { gat_issue(v, f); gat_finish<SLEEP>(v, f, miss); }
typedef __attribute__((address_space(3))) float lfloat;
DI unsigned ldsa(const float* p) { return (unsigned)(uintptr_t)(const lfloat*)p; }
DI void dma_nt(unsigned lds, int voff, v4u srd, int soff) {
  unsigned keep;
  asm volatile("s_mov_b32 %0, m0\n\ts_mov_b32 m0, %1\n\ts_nop 0\n\tbuffer_load_dwordx4 %2, %3, %4 offen nt lds\n\ts_mov_b32 m0, %0"
               : "=&s"(keep) : "s"(lds), "v"(voff), "s"(srd), "s"(soff) : "memory");
}
DI unsigned now() { return (unsigned)__builtin_amdgcn_s_memrealtime(); }
DI void pace(unsigned t0, unsigned due) {
#pragma unroll 1
  for (int it = 0; it < 2048; ++it) {
    if ((int)(now() - t0) >= (int)due) break;
    __builtin_amdgcn_s_sleep(1);
  }
}
struct Miss {
  DI bool operator()(v4u x) const { return umax4(x) == SENT; }
  DI bool operator()(v2u x) const { return max(x.x, x.y) == SENT; }
  DI bool operator()(unsigned x) const { return x == SENT; }
};
DI void tr8(const v4u (&w)[8], v4u (&f)[8]) {
#pragma unroll
  for (int m = 0; m < 4; ++m)
#pragma unroll
    for (int p = 0; p < 4; ++p) {
      f[2 * m][p] = __builtin_amdgcn_perm(w[2 * p + 1][m], w[2 * p][m], 0x05040100u);
      f[2 * m + 1][p] = __builtin_amdgcn_perm(w[2 * p + 1][m], w[2 * p][m], 0x07060302u);
    }
}
DI void kfold_store(const f32x4 (&d)[8], float* slot, int lane) {
  const bool lo = (lane & 8) == 0;
  float o[8];
#pragma unroll
  for (int j = 0; j < 8; ++j) {
    float x = lo ? d[j][0] : d[j][3], y = lo ? d[j][2] : d[j][1];
    o[j] = x + dppf<0x128>(y);
  }
  *(f32x4*)(slot + lane * 8) = f32x4{o[0], o[1], o[2], o[3]};
  *(f32x4*)(slot + lane * 8 + 4) = f32x4{o[4], o[5], o[6], o[7]};
}
DI float sum8(const float* p, int stride) {
  return ((p[0] + p[stride]) + (p[2 * stride] + p[3 * stride])) + ((p[4 * stride] + p[5 * stride]) + (p[6 * stride] + p[7 * stride]));
}
DI f32x2 sum8x2(const float* p, int stride) {
  auto L = [&](int i) { return *(const f32x2*)(p + i * stride); };
  return ((L(0) + L(1)) + (L(2) + L(3))) + ((L(4) + L(5)) + (L(6) + L(7)));
}
DI v4u canon4(f32x4 s) { return v4u{canon(fbits(s[0])), canon(fbits(s[1])), canon(fbits(s[2])), canon(fbits(s[3]))}; }
DI f32x4 asf(v4u x) { return __builtin_bit_cast(f32x4, x); }
DI v4u silu8(v4u c) {
  v4u r;
#pragma unroll
  for (int m = 0; m < 4; ++m) {
    float a = bflo(c[m]), b = bfhi(c[m]);
    a = a * rcp(1.f + ex2(-a * LOG2E)); b = b * rcp(1.f + ex2(-b * LOG2E));
    r[m] = pk2(a, b);
  }
  return r;
}

struct Args {
  const u16 *x, *kc, *vc, *wq, *wo, *wg, *wu, *wd, *c, *wmod;
  u16* out;
  char* ws;
};

#define STAMPW(i) do { } while (0)
#define STAMP(i) do { } while (0)

template <int I> struct IC { static constexpr int v = I; };
template <int I, int N, class F> DI void sfor(F&& f) { if constexpr (I < N) { f(IC<I>{}); sfor<I + 1, N>(f); } }
constexpr int imax(int a, int b) { return a > b ? a : b; }
constexpr int win(int i) { return i < S1 ? W1 : WIN; }
constexpr int landed(int j) { int i = j + 1; while (i < NS && i - win(i) < j) ++i; return i; }
constexpr int ktr_slot(int t, int m) { return landed(NP + 4 * t + 3) + (t < 7 ? m : m / 2); }
constexpr int qk_slot(int t) { return imax(ktr_slot(t, 3) + (t < 7 ? 1 : 0), QSL + t / 2); }
constexpr int EXP0 = qk_slot(7) + 1;
constexpr int PV0 = EXP0 + 3;
constexpr int imin(int a, int b) { return a < b ? a : b; }
constexpr int pv_slot(int blk, int m) { return imin(imax(PV0 + 2 * blk, landed(NP + 39 + 8 * blk)) + m / 2, NS); }
constexpr int RW = NP * 256;
static_assert(RW >= 1536 && PV0 <= NS, "schedule");

__global__ void __launch_bounds__(768, 3) mega(Args a) {
  __shared__ __attribute__((aligned(16))) unsigned cnt[C_N];
  __shared__ __attribute__((aligned(16))) unsigned qs[8 * 68];
  __shared__ __attribute__((aligned(16))) float redO[8 * 1056];
  __shared__ __attribute__((aligned(16))) float redC[4 * 1056];
  __shared__ __attribute__((aligned(16))) float mlw[8 * 16];
  __shared__ __attribute__((aligned(16))) float mlf[16];
  __shared__ __attribute__((aligned(1024))) float reg[8 * RW];
  __shared__ unsigned tsx[8];
  float* const redT = reg + 1024;
  float* const redU = reg + 2048;

  const int bid = blockIdx.x, lane = threadIdx.x & 63;
  const int w = __builtin_amdgcn_readfirstlane(threadIdx.x >> 6);
  const int xc = bid & 7, jx = bid >> 3;
  const int ab = xc, akvh = jx >> 3, as = jx & 7;
  const int grp = ab * 4 + akvh;
  const bool typeB = jx >= 16;
  const int jb = jx - 16;
  const int g = lane >> 4, n = lane & 15;
  const int arow = (n >> 1) * 2048 + (n & 1) * 64 + g * 16;
  const bool doT3 = jx < 14, doT4 = typeB && jb < 14;
  const rsrc_t rws = mk(a.ws, WS_BYTES);
  const Miss miss;
  if (threadIdx.x < C_N) cnt[threadIdx.x] = 0;
  const unsigned tstart = now();
  const int ta = xc * 14 + jx;
  const int wl = (n >> 3) * 32 + g * 8, wc = (n & 7) * 16;

  if (w < 8) {
    if (w == 0) STAMP(0);
    {
      const v4u S = {SENT, SENT, SENT, SENT};
      if (!(ABL & 1)) st_sc(S, rws, lane * 16, WS_ARENA + bid * ARENA_PER + w * 1024);
      if (!(ABL & 1)) st_sc(S, rws, lane * 16, WS_ARENA + bid * ARENA_PER + (w < 3 ? 8 + w : w) * 1024);
    }
    const rsrc_t rk = mk(a.kc, 64u << 20), rv = mk(a.vc, 64u << 20);
    const int kvbase = (grp * 8 + as) * (256 << 10) + w * (LAY ? 1024 : 32 << 10);
    const int koff = kvbase + ((lane >> 2) & 3) * 256 + g * 64 + (lane & 3) * 16;
    const int voff = kvbase + lane * 16;
    const bool l0 = lane & 1, l1 = lane & 2;
    const int td = xc * 14 + jb;
    v4u k[32], v[32], wt[16], qf[4], P[4];
    f32x4 S[8], O[8];
    float mw = -INFINITY, lw = 0.f, nm = 0.f;
    auto ktr = [&](int t, int m) {
      unsigned r0 = k[4 * t][m], r1 = k[4 * t + 1][m], r2 = k[4 * t + 2][m], r3 = k[4 * t + 3][m];
      const unsigned d0 = dppu<0xB1>(r0), d1 = dppu<0xB1>(r1), d2 = dppu<0xB1>(r2), d3 = dppu<0xB1>(r3);
      const unsigned s0 = l0 ? d1 : r0, s1 = l0 ? r1 : d0, s2 = l0 ? d3 : r2, s3 = l0 ? r3 : d2;
      const unsigned e0 = dppu<0x4E>(s0), e1 = dppu<0x4E>(s1), e2 = dppu<0x4E>(s2), e3 = dppu<0x4E>(s3);
      k[4 * t][m] = l1 ? e2 : s0; k[4 * t + 2][m] = l1 ? s2 : e0;
      k[4 * t + 1][m] = l1 ? e3 : s1; k[4 * t + 3][m] = l1 ? s3 : e1;
      asm volatile("" : "+v"(k[4 * t]), "+v"(k[4 * t + 1]), "+v"(k[4 * t + 2]), "+v"(k[4 * t + 3]));
      SB0();
    };
    auto qk = [&](int t) {
      f32x4 s = {0.f, 0.f, 0.f, 0.f};
#pragma unroll
      for (int i = 0; i < 4; ++i) s = mfma(k[4 * t + i], qf[i], s);
      unsigned p0 = pk2(s[0], s[1]), p1 = pk2(s[2], s[3]);
      const float sc = 0.08838834764831845f;
      p0 = pk2(bflo(p0) * sc, bfhi(p0) * sc); p1 = pk2(bflo(p1) * sc, bfhi(p1) * sc);
      S[t] = f32x4{bflo(p0), bfhi(p0), bflo(p1), bfhi(p1)};
      mw = fmaxf(mw, fmaxf(fmaxf(S[t][0], S[t][1]), fmaxf(S[t][2], S[t][3])));
      asm volatile("" : "+v"(S[t]), "+v"(mw));
    };
    auto expo = [&](int t) {
      float e0 = ex2(fmaf(S[t][0], LOG2E, nm)), e1 = ex2(fmaf(S[t][1], LOG2E, nm)), e2 = ex2(fmaf(S[t][2], LOG2E, nm)), e3 = ex2(fmaf(S[t][3], LOG2E, nm));
      lw += (e0 + e1) + (e2 + e3);
      unsigned p0 = pk2(e0, e1), p1 = pk2(e2, e3);
      asm volatile("" : "+v"(p0), "+v"(p1), "+v"(lw));
      P[t >> 1][(t & 1) * 2] = p0; P[t >> 1][(t & 1) * 2 + 1] = p1;
    };
    auto pv = [&](int blk, int m) {
      v4u f0, f1;
#pragma unroll
      for (int p = 0; p < 4; ++p) {
        f0[p] = __builtin_amdgcn_perm(v[8 * blk + 2 * p + 1][m], v[8 * blk + 2 * p][m], 0x05040100u);
        f1[p] = __builtin_amdgcn_perm(v[8 * blk + 2 * p + 1][m], v[8 * blk + 2 * p][m], 0x07060302u);
      }
      const f32x4 z = {0.f, 0.f, 0.f, 0.f};
      O[2 * m] = mfma(f0, P[blk], blk ? O[2 * m] : z);
      O[2 * m + 1] = mfma(f1, P[blk], blk ? O[2 * m + 1] : z);
      asm volatile("" : "+v"(O[2 * m]), "+v"(O[2 * m + 1]));
      SB0();
    };
    SB0();
    auto pos = [&](int j) { return j * (LAY ? 8192 : 1024); };
    const u64 pb0 = (u64)a.wmod, pb1 = typeB ? (u64)a.wo : (ta >= 56 ? (u64)a.wu : (u64)a.wg);
    const v4u srd0 = {(unsigned)pb0, (unsigned)(pb0 >> 32) & 0xffffu, 12u << 20, 0x00020000u};
    const v4u srd1 = {(unsigned)pb1, (unsigned)(pb1 >> 32) & 0xffffu, typeB ? 8u << 20 : (doT3 ? 7u << 20 : 0u), 0x00020000u};
    const int ps1 = typeB ? 2048 : 7168;
    const int pv0 = ((bid >> 6) * 256 + (w >> 1) * 64 + (w & 1) * 4 + wl) * 12288 + 4096 + (bid & 63) * 128 + wc;
    const int pv1 = typeB ? ((2 * xc + (jb >> 3)) * 256 + w * 32 + g * 8) * 2048 + (jb & 7) * 256 + n * 16 : (w * 128 + wl) * 7168 + (ta % 56) * 128 + wc;
    const unsigned park = ldsa(reg + w * RW);
    const rsrc_t rwl = mk((const void*)pb1, doT3 ? 7u << 20 : 0u);
    const unsigned tw = tstart + (w * TAU) / 128;
    unsigned tnow = tw;
    sfor<0, NS + 1>([&](auto ic) __attribute__((always_inline)) {
      constexpr int i = decltype(ic)::v;
      if constexpr (i >= W1 && i < NS) VMWAIT(win(i) - 1);
      if constexpr (TAU && i >= P0 && i < NS) {
        constexpr unsigned due = PA + (i * TAU) / 16;
        if ((int)(tnow - tw) < (int)due) pace(tw, due);
      }
      SB0();
      if constexpr (i < 4) { if (ABL & 2) dma_nt(park + i * 1024, (4 << 20) + bid * 32768 + w * 1024 + lane * 16, srd0, i * 8192); else dma_nt(park + i * 1024, pv0, srd0, i * 12288); }
      else if constexpr (i < NP) { if (ABL & 2) dma_nt(park + i * 1024, (typeB ? xc * 16 + jb : ta % 56) * 65536 + w * 1024 + lane * 16, srd1, (i - 4) * 8192); else dma_nt(park + i * 1024, pv1, srd1, (i - 4) * ps1); }
      else if constexpr (i < NP + 32) k[i - NP] = ld_nt(rk, koff, pos(i - NP));
      else if constexpr (i < NS) v[i - NP - 32] = ld_nt(rv, voff, pos(i - NP - 32));
      SB0();
      if constexpr (TAU && i + 1 >= P0 && i + 1 < NS) { tnow = now(); SB0(); }
      if constexpr (i == P0 - 1) {
        asm volatile("s_waitcnt lgkmcnt(0)\n\ts_barrier\n\ts_waitcnt vmcnt(%0)" :: "n"(P0) : "memory");
        lds_arrive(&cnt[C_RESET], lane);
        SB0();
      }
      if constexpr (i == landed(NP - 1)) lds_arrive(&cnt[C_PARK], lane);
      if constexpr (i % 12 == 3) STAMPW(i / 12);
      if constexpr (DIAG == 5 && i == 15) { if (w == 0 && lane == 0) tsx[5] = now(); }
      if constexpr (DIAG == 6 && i == 45) { if (w == 0 && lane == 0) tsx[6] = now(); }
      SB0();
#pragma unroll
      for (int t = 0; t < 8; ++t)
#pragma unroll
        for (int m = 0; m < 4; ++m)
          if (ktr_slot(t, m) == i) ktr(t, m);
      if constexpr (i == QSL) {
        if (w == 0) STAMP(1);
        lds_wait<2>(&cnt[C_QG], 4);
        if (w == 0) STAMP(2);
#pragma unroll
        for (int j = 0; j < 4; ++j) qf[j] = *(const v4u*)(qs + (n & 7) * 68 + g * 16 + j * 4);
      }
#pragma unroll
      for (int t = 0; t < 8; ++t)
        if (qk_slot(t) == i) qk(t);
      if constexpr (i == EXP0 - 1) { mw = max32(max16(mw)); nm = -mw * LOG2E; }
      if constexpr (i >= EXP0 && i < EXP0 + 3) { expo(3 * (i - EXP0)); expo(3 * (i - EXP0) + 1); if constexpr (i < EXP0 + 2) expo(3 * (i - EXP0) + 2); }
      if constexpr (i == EXP0 + 2) {
        lw = add32(add16(lw));
        if (lane < 8) *(f32x2*)(mlw + w * 16 + lane * 2) = f32x2{mw, lw};
        CBAR();
        unsigned old = 0;
        if (lane == 0) old = __hip_atomic_fetch_add(&cnt[C_MLW], 1u, __ATOMIC_RELAXED, __HIP_MEMORY_SCOPE_WORKGROUP);
        old = __builtin_amdgcn_readfirstlane(old);
        CBAR();
        if (old == 7) {
          f32x2 r = *(const f32x2*)(mlw + lane * 2);
          float M = max32(max16(max8(r[0])));
          float L = add32(add16(add8(r[1] * ex2((r[0] - M) * LOG2E))));
          if (lane < 8) {
            *(f32x2*)(mlf + lane * 2) = f32x2{M, L};
            st_sc2(v2u{canon(fbits(M)), canon(fbits(L))}, rws, lane * 512, WS_ML + akvh * 4096 + ab * 64 + as * 8);
          }
          lds_arrive(&cnt[C_MLF], lane);
        }
        if (w == 0) STAMP(3);
      }
#pragma unroll
      for (int blk = 0; blk < 3; ++blk)
#pragma unroll
        for (int m = 0; m < 4; ++m)
          if (pv_slot(blk, m) == i) pv(blk, m);
      SB0();
    });
    float fs;
    {
      lds_wait<1>(&cnt[C_MLF], 1);
      f32x2 ml = *(const f32x2*)(mlf + (n & 7) * 2);
      fs = ex2((mw - ml[0]) * LOG2E) * rcp(ml[1]);
      asm volatile("" : "+v"(fs));
    }
    SB0();
#pragma unroll
    for (int i = 0; i < WE; ++i) wt[8 + i] = ld_nt(rwl, pv1, (64 + i) * 7168);
    SB0();
#pragma unroll
    for (int m = 0; m < 4; ++m) pv(3, m);
    if (w == 0) STAMP(4);
    if (w == 7) STAMP(26);
    STAMPW(7);
    {
      if (n < 8) {
        float* dst = redO + w * 1056 + n * 132 + g * 32;
#pragma unroll
        for (int r = 0; r < 4; ++r) {
          *(f32x4*)(dst + r * 8) = f32x4{O[0][r] * fs, O[1][r] * fs, O[2][r] * fs, O[3][r] * fs};
          *(f32x4*)(dst + r * 8 + 4) = f32x4{O[4][r] * fs, O[5][r] * fs, O[6][r] * fs, O[7][r] * fs};
        }
      }
      lds_arrive(&cnt[C_O], lane);
      lds_wait<0>(&cnt[C_O], 8);
      const f32x2 s = sum8x2(redO + w * 132 + lane * 2, 1056);
      st_sc1(canon(pk2(s[0], s[1])), rws, g * 4096 + n * 4, WS_OP + (akvh * 8 + w) * 16384 + (as >> 1) * 1024 + ab * 128 + (as & 1) * 64);
    }
    SB0();
    if (w == 0) STAMP(5);
    v4u f[8];
    if (doT4) {
      {
        f32x4 d[8];
        const int ks = td >> 4, c16 = td & 15;
        const rsrc_t rw = mk(a.wd, 7u << 20);
        const int off = (ks * 512 + w * 64 + (n >> 3) * 32 + g * 8) * 2048 + c16 * 128 + (n & 7) * 16;
        SB0();
        if (WDG) lds_wait<4>(&cnt[C_T1], WDG);
        SB0();
#pragma unroll
        for (int i = 0; i < 8; ++i) { if (i >= WDW) VMWAIT(WDW - 1); SB0(); wt[i] = ld_nt(rw, off, i * 2048); SB0(); }
        SB0();
        tr8((const v4u(&)[8])wt[0], f);
        PIN8(f);
        if (w == 0) STAMP(12);
        lds_wait<1>(&cnt[C_PRE], 1);
        v4u gu[2];
        gather<0>(gu, [&](int i) { return ld_sc(rws, n * 64 + g * 16, WS_GU + (ks * 8 + w) * 1024 + i * 56 * 1024); }, miss);
        if (w == 0) STAMP(13);
        v4u hh;
#pragma unroll
        for (int m = 0; m < 4; ++m) hh[m] = pk2(bflo(gu[0][m]) * bflo(gu[1][m]), bfhi(gu[0][m]) * bfhi(gu[1][m]));
#pragma unroll
        for (int j = 0; j < 8; ++j) d[j] = mfma(hh, f[j], f32x4{0.f, 0.f, 0.f, 0.f});
        kfold_store(d, redU + w * RW, lane);
        lds_arrive(&cnt[C_T4], lane);
        lds_wait<0>(&cnt[C_T4], 8);
        const float t = sum8(redU + w * 64 + lane, RW);
        st_sc1(canon(fbits(t)), rws, lane * 4, WS_DP + (w * 16 + c16) * 2048 + ks * 256);
        if (w == 0) STAMP(14);
      }
    } else if (doT3) {
      v4u sbcb[4];
      auto aux_ld = [&](int i) { return ld_sc(rws, arow, WS_SB + w * 256 + (i >> 1) * 16384 + (i & 1) * 128); };
      gat_issue(sbcb, aux_ld);
#pragma unroll
      for (int i = WE; i < 8; ++i) { if (i >= WGW) VMWAIT(WGW - 1); SB0(); wt[8 + i] = ld_nt(rwl, pv1, (64 + i) * 7168); SB0(); }
#pragma unroll
      for (int i = 0; i < 8; ++i) wt[i] = *(const v4u*)(reg + w * RW + 1024 + i * 256 + lane * 4);
      v4u f1[8];
      tr8((const v4u(&)[8])wt[0], f);
      PIN8(f);
      SB0();
      tr8((const v4u(&)[8])wt[8], f1);
      PIN8(f1);
      if (w == 0) STAMP(31);
      SB0();
      gat_finish<1>(sbcb, aux_ld, miss);
      f32x4 d[8];
#pragma unroll
      for (int j = 0; j < 8; ++j) d[j] = mfma(sbcb[1], f1[j], mfma(sbcb[0], f[j], f32x4{0.f, 0.f, 0.f, 0.f}));
      kfold_store(d, redT + w * RW, lane);
#pragma unroll
      for (int j = 0; j < 8; ++j) d[j] = mfma(sbcb[3], f1[j], mfma(sbcb[2], f[j], f32x4{0.f, 0.f, 0.f, 0.f}));
      kfold_store(d, redT + 512 + w * RW, lane);
      lds_arrive(&cnt[C_C12], lane);
      lds_wait<1>(&cnt[C_C12], 8);
      float c2 = sum8(redT + w * 64 + lane, RW), c1 = sum8(redT + 512 + w * 64 + lane, RW);
      asm volatile("" : "+v"(c1), "+v"(c2));
      if (w == 0) STAMP(9);
      lds_wait<1>(&cnt[C_PRE], 1);
      v4u xr[2];
      {
        const int sb0 = WS_X1 + xc * REPX;
        gather<0>(xr, [&](int i) { return ld_sc(rws, arow, sb0 + w * 256 + i * 128); }, miss);
      }
      if (w == 0) STAMP(10);
#pragma unroll
      for (int j = 0; j < 8; ++j) d[j] = mfma(xr[1], f1[j], mfma(xr[0], f[j], f32x4{0.f, 0.f, 0.f, 0.f}));
      kfold_store(d, redU + w * RW, lane);
      lds_arrive(&cnt[C_T3], lane);
      lds_wait<0>(&cnt[C_T3], 9);
      const f32x2 st = *(const f32x2*)(mlw + w * 2);
      float t = fmaf(fmaf(st[0], c1, sum8(redU + w * 64 + lane, RW)), st[1], c2);
      if (ta < 56) { t = rbf(t); t = t > 0.f ? t : t * 0.f; }
      const unsigned u = canon(pk2(t, dppf<0xB1>(t)));
      if (!l0) st_sc1(u, rws, lane * 2, WS_GU + ta * 1024 + w * 128);
      if (w == 0) STAMP(11);
    }
  } else {
    const int cw = w - 8;
    __builtin_amdgcn_s_setprio(2);
    const u64 ep = *(const __attribute__((address_space(4))) u64*)(uintptr_t)a.ws;
    const u64 tag = ep + 0x9E3779B97F4A7C15ull;
    unsigned tlo = (unsigned)tag, thi = (unsigned)(tag >> 32);
    const rsrc_t rc = mk(a.c, 16384), rm = mk(a.wmod, 12u << 20), rx = mk(a.x, 16384);
    const int wlane_r = wl, wlane_c = wc;
    unsigned tdiag = 0;
    if (cw >= 2) {
      const int h = cw - 2;
      const int ct = bid & 31, ks = bid >> 5;
      const int qct = bid & 63, qks = bid >> 6;
      v4u wm[8], wq0[8], wq1[8];
      const v4u cv = ld_pl(rc, arow, (ks * 128 + h * 64) * 2);
      {
        const int off = (ks * 128 + h * 64 + wlane_r) * 12288 + ct * 128 + wlane_c;
#pragma unroll
        for (int i = 0; i < 8; ++i) wm[i] = ld_nt(rm, off, i * 12288);
      }
      SB0();
      auto wq_ld = [&]() {
        const rsrc_t rq = mk(a.wq, 8u << 20);
        const int off = (qks * 256 + h * 128 + wlane_r) * 8192 + qct * 128 + wlane_c;
#pragma unroll
        for (int i = 0; i < 8; ++i) wq0[i] = ld_nt(rq, off, i * 8192);
#pragma unroll
        for (int i = 0; i < 8; ++i) wq1[i] = ld_nt(rq, off, (64 + i) * 8192);
      };
      wq_ld();
      SB0();
      asm volatile("" : "+s"(tlo), "+s"(thi));
      asm volatile("s_waitcnt lgkmcnt(0)\n\ts_barrier" ::: "memory");
      auto tmiss = [&](v4u x) { return ((x.y ^ tlo) | (x.w ^ thi)) != 0; };
      const v4u ca = silu8(cv);
      v4u f[8];
      f32x4 d[8];
      tr8(wm, f);
#pragma unroll
      for (int j = 0; j < 8; ++j) d[j] = mfma(ca, f[j], f32x4{0.f, 0.f, 0.f, 0.f});
      kfold_store(d, redC + h * 512, lane);
      lds_arrive(&cnt[C_M1], lane);
      lds_wait<0>(&cnt[C_M1], 2);
      {
        const float* p = redC + h * 256 + lane * 2;
        const f32x2 s0 = *(const f32x2*)p + *(const f32x2*)(p + 512), s1 = *(const f32x2*)(p + 128) + *(const f32x2*)(p + 640);
        st_sc(v4u{fbits(s0[0]), tlo, fbits(s0[1]), thi}, rws, lane * 16, WS_MPA + (ct * 8 + ks) * MPA_SLOT + h * 2048);
        st_sc(v4u{fbits(s1[0]), tlo, fbits(s1[1]), thi}, rws, lane * 16, WS_MPA + (ct * 8 + ks) * MPA_SLOT + h * 2048 + 1024);
      }
      lds_arrive(&cnt[C_M1S], lane);
      if (cw == 2) STAMP(16);
      SB0();
      v4u f2[8];
      tr8(wq0, f);
      tr8(wq1, f2);
      PIN8(f);
      PIN8(f2);
      if (cw == 2) STAMP(28);
      lds_wait<1>(&cnt[C_R1], 1);
      v4u ha[4];
      gather<1>(ha, [&](int i) { return ld_sc(rws, (n >> 1) * 4096 + (n & 1) * 64 + g * 16, WS_H + xc * HREP + (qks * 256 + h * 128) * 4 + i * 128); }, tmiss);
      if (cw == 2) STAMP(19);
      const v4u a0 = {ha[0].x, ha[0].z, ha[1].x, ha[1].z}, a1 = {ha[2].x, ha[2].z, ha[3].x, ha[3].z};
#pragma unroll
      for (int j = 0; j < 8; ++j) d[j] = mfma(a1, f2[j], mfma(a0, f[j], f32x4{0.f, 0.f, 0.f, 0.f}));
      kfold_store(d, redC + 1024 + h * 512, lane);
      lds_arrive(&cnt[C_Q], lane);
      lds_wait<0>(&cnt[C_Q], 2);
      {
        f32x4 s = *(const f32x4*)(redC + 1024 + h * 256 + lane * 4) + *(const f32x4*)(redC + 1536 + h * 256 + lane * 4);
        lds_wait<1>(&cnt[C_GO], 1);
        st_sc(canon4(s), rws, g * 65536 + n * 16, WS_QP + h * 4 * 65536 + (qct >> 4) * 16384 + qks * 4096 + (qct & 15) * 256);
      }
      lds_arrive(&cnt[C_QS], lane);
      if (cw == 2) STAMP(20);
    } else {
      v4u xr[2];
      unsigned xp = 0;
      if (PREF && cw == 0) {
        u64 pc;
        unsigned len;
        asm volatile("s_getpc_b64 %0\n.Lref%=:\n\ts_mov_b32 %1, .Lref%=-_Z4mega4Args" : "=s"(pc), "=s"(len));
        pc -= len;
        asm volatile("s_mov_b32 %0, _Z8mega_endv-_Z4mega4Args" : "=s"(len));
        const rsrc_t rcode = mk((const void*)pc, len);
        xr[0] = ld_pl(rcode, lane * 16, jx * 1024); xr[1] = ld_pl(rcode, lane * 16, jx * 1024 + 32768);
      }
      if (cw == 1) {
        xr[0] = ld_pl(rx, lane * 16, xc * 2048); xr[1] = ld_pl(rx, lane * 16, xc * 2048 + 1024);
        xp = ld_pl1(rx, n * 4, xc * 2048 + jx * 64);
      }
      asm volatile("" : "+s"(tlo), "+s"(thi));
      asm volatile("s_waitcnt lgkmcnt(0)\n\ts_barrier" ::: "memory");
      if (cw == 0) {
        lds_wait<1>(&cnt[C_RESET], 8);
        if (PREF) asm volatile("" :: "v"(xr[0]), "v"(xr[1]));
        if (lane < 8) st_sc2(v2u{tlo, thi}, rws, lane * FLAG_REP, WS_FLAGS + bid * 8);
        STAMP(15);
        lds_wait<4>(&cnt[C_R1], 1);
        v4u fl[2];
        gather<4>(fl, [&](int i) { return ld_sc(rws, lane * 16, WS_FLAGS + xc * FLAG_REP + i * 1024); },
                  [&](v4u x) { return ((x.x ^ tlo) | (x.y ^ thi) | (x.z ^ tlo) | (x.w ^ thi)) != 0; });
        lds_arrive(&cnt[C_GO], lane);
        if (bid == 0 && lane == 0) st_sc2(v2u{tlo, thi}, rws, 0, WS_EPOCH);
        STAMP(17);
      } else {
        float ln0, ln1;
        {
          const float x0 = bitsf(__builtin_amdgcn_readfirstlane(xr[0][0] << 16));
          float s = 0.f, q = 0.f;
#pragma unroll
          for (int i = 0; i < 8; ++i) {
            const float d0 = bflo(xr[i >> 2][i & 3]) - x0, d1 = bfhi(xr[i >> 2][i & 3]) - x0;
            s += d0 + d1; q = fmaf(d0, d0, fmaf(d1, d1, q));
          }
          s = wave_sum(s); q = wave_sum(q);
          const float md = s * (1.f / 1024.f);
          const float var = fmaxf(q * (1.f / 1024.f) - md * md, 0.f);
          const float mean = rbf(x0 + md);
          const float den = rbf(__builtin_sqrtf(rbf(rbf(var) + 1e-5f)));
          ln0 = rbf(rbf(bflo(xp) - mean) / den); ln1 = rbf(rbf(bfhi(xp) - mean) / den);
          asm volatile("" : "+v"(ln0), "+v"(ln1));
        }
        lds_wait<1>(&cnt[C_M1S], 2);
        v4u mp[4];
        gather<1>(mp, [&](int i) { return ld_sc(rws, g * MPA_SLOT + n * 16, WS_MPA + (jx >> 1) * 8 * MPA_SLOT + xc * 512 + (jx & 1) * 256 + (i >> 1) * 128 * MPA_SLOT + (i & 1) * 4 * MPA_SLOT); },
                  [&](v4u x) { return ((x.y ^ tlo) | (x.w ^ thi)) != 0; });
        const float sa0 = rbf(add32(add16(bitsf(mp[0].x) + bitsf(mp[1].x)))), sa1 = rbf(add32(add16(bitsf(mp[0].z) + bitsf(mp[1].z))));
        const float ca0 = rbf(add32(add16(bitsf(mp[2].x) + bitsf(mp[3].x)))), ca1 = rbf(add32(add16(bitsf(mp[2].z) + bitsf(mp[3].z))));
        const unsigned u = pk2(rbf(ln0 * rbf(1.f + ca0)) + sa0, rbf(ln1 * rbf(1.f + ca1)) + sa1);
        st_sc(v4u{dppu<0xA0>(u), tlo, dppu<0xF5>(u), thi}, rws, (g * 2 + (n & 1)) * HREP + ((n >> 1) & 1) * 128 + (n >> 2) * 16, WS_H + xc * 4096 + (jx >> 1) * 256 + (jx & 1) * 64);
        lds_arrive(&cnt[C_R1], lane);
        STAMP(18);
      }
    }
    lds_wait<1>(&cnt[C_QS], 2);
    {
      v4u qp[4];
      gather<1>(qp, [&](int i) { return ld_sc(rws, lane * 16, WS_QP + grp * 16384 + cw * 1024 + i * 4096); }, miss);
      f32x4 s = (asf(qp[0]) + asf(qp[1])) + (asf(qp[2]) + asf(qp[3]));
      *(v2u*)(qs + (cw * 2 + (lane >> 5)) * 68 + (lane & 31) * 2) = v2u{pk2(s[0], s[1]), pk2(s[2], s[3])};
      lds_arrive(&cnt[C_QG], lane);
      if (DIAG == 2) tdiag = now();
      if (cw == 0) STAMP(21);
    }
    const bool red = (jx & 14) == 14;
    const int cblk = ((jx >> 4) * 2 + (jx & 1)) * 4 + cw, unit = xc * 16 + cblk, cp = lane & 31, half = lane >> 5;
    auto add2 = [](v2u p, v2u q) { return f32x2{bitsf(p.x) + bitsf(q.x), bitsf(p.y) + bitsf(q.y)}; };
    auto a32 = [](f32x2 s) { return f32x2{add32(s[0]), add32(s[1])}; };
    v4u f0[8], f1[8];
    __builtin_amdgcn_s_setprio(0);
    {
      const int ct = bid & 63, ks = bid >> 6;
      const v4u c0 = ld_pl(rc, arow, (ks * 256 + cw * 64) * 2);
      lds_wait<2>(&cnt[C_PARK], 8);
      const float* pk = reg + 2 * cw * RW + lane * 4;
      v4u w0[8], w1[8];
#pragma unroll
      for (int i = 0; i < 8; ++i) w0[i] = *(const v4u*)(pk + (i >> 2) * RW + (i & 3) * 256);
      const v4u a0 = silu8(c0);
      f32x4 d[8];
      tr8(w0, f0);
#pragma unroll
      for (int j = 0; j < 8; ++j) d[j] = mfma(a0, f0[j], f32x4{0.f, 0.f, 0.f, 0.f});
      kfold_store(d, redC + 2048 + cw * 512, lane);
      lds_arrive(&cnt[C_M2], lane);
#pragma unroll
      for (int i = 0; i < 8; ++i) { w0[i] = *(const v4u*)(pk + 1024 + i * 256); w1[i] = *(const v4u*)(pk + RW + 1024 + i * 256); }
      lds_wait<0>(&cnt[C_M2], 4);
      const float* src = redC + 2048 + (lane >> 5) * 1024 + cw * 128 + (lane & 31) * 4;
      f32x4 s = *(const f32x4*)src + *(const f32x4*)(src + 512);
      s = f32x4{add32(s[0]), add32(s[1]), add32(s[2]), add32(s[3])};
      if (lane < 32) st_sc(canon4(s), rws, g * 65536 + n * 16, WS_MPB + cw * 131072 + (ct & 15) * 4096 + (ct >> 4) * 1024 + ks * 256);
      if (cw == 0) STAMP(22);
      tr8(w0, f0);
      PIN8(f0);
      tr8(w1, f1);
      PIN8(f1);
    }
    unsigned xo = 0;
    f32x2 ga, gb, cb1;
    if (red) {
      xo = ld_pl1(rx, cp * 4, xc * 2048 + cblk * 128);
      v2u mp[8];
      gather<8>(mp, [&](int j) { return ld_sc2(rws, half * 512 + cp * 8, WS_MPB + unit * 4096 + (j >> 1) * 1024 + (j & 1) * 256); }, miss);
      ga = a32(add2(mp[0], mp[1])); ga = f32x2{rbf(ga[0]), rbf(ga[1])};
      const f32x2 sb = a32(add2(mp[2], mp[3]));
      cb1 = a32(add2(mp[4], mp[5])); cb1 = f32x2{rbf(1.f + rbf(cb1[0])), rbf(1.f + rbf(cb1[1]))};
      gb = a32(add2(mp[6], mp[7])); gb = f32x2{rbf(gb[0]), rbf(gb[1])};
      const f32x2 z = half ? cb1 : sb;
      st_sc1(canon(pk2(z[0], z[1])), rws, half * 16384 + cp * 4, WS_SB + xc * 2048 + cblk * 128);
      if (cw == 0) STAMP(24);
    }
    if (typeB) {
      const int hp = 2 * xc + (jb >> 3), cg = jb & 7, head = 2 * hp + (cw >> 1);
      if (cw == 0) STAMP(27);
      f32x4 F;
      {
        lds_wait<8>(&cnt[C_MLF], 1);
        v4u ml[1];
        gather<8>(ml, [&](int) { return ld_sc(rws, (n >> 1) * 64 + g * 16, WS_ML + head * 512); }, miss);
        const float M = max32(max16(fmaxf(bitsf(ml[0].x), bitsf(ml[0].z))));
        const float w0 = bitsf(ml[0].y) * ex2((bitsf(ml[0].x) - M) * LOG2E), w1 = bitsf(ml[0].w) * ex2((bitsf(ml[0].z) - M) * LOG2E);
        const float mine = ((lane & 1) ? w1 : w0) * rcp(add32(add16(w0 + w1)));
        const auto s = __builtin_amdgcn_permlane16_swap(fbits(mine), fbits(mine), false, false);
        const auto e = __builtin_amdgcn_permlane32_swap(s[0], s[0], false, false), o = __builtin_amdgcn_permlane32_swap(s[1], s[1], false, false);
        F = f32x4{bitsf(e[0]), bitsf(o[0]), bitsf(e[1]), bitsf(o[1])};
      }
      asm volatile("" : "+v"(F));
      __builtin_amdgcn_s_setprio(2);
      lds_wait<2>(&cnt[C_O], 8);
      v4u oa[8];
      gather<0>(oa, [&](int q) { return ld_sc(rws, n * 64 + g * 16, WS_OP + head * 16384 + (cw & 1) * 8192 + q * 1024); }, miss);
      if (cw == 0) STAMP(6);
      v4u oo[2];
#pragma unroll
      for (int j = 0; j < 2; ++j)
#pragma unroll
        for (int m = 0; m < 4; ++m) {
          float lo = bflo(oa[4 * j][m]) * F[0], hi = bfhi(oa[4 * j][m]) * F[0];
#pragma unroll
          for (int p = 1; p < 4; ++p) { lo = fmaf(bflo(oa[4 * j + p][m]), F[p], lo); hi = fmaf(bfhi(oa[4 * j + p][m]), F[p], hi); }
          oo[j][m] = pk2(lo + dppf<0xB1>(lo), hi + dppf<0xB1>(hi));
        }
      f32x4 d[8];
      float* dst = redC + cw * 1056 + g * 264 + n * 8;
#pragma unroll
      for (int j = 0; j < 8; ++j) d[j] = mfma(oo[1], f1[j], mfma(oo[0], f0[j], f32x4{0.f, 0.f, 0.f, 0.f}));
      *(f32x4*)(dst) = f32x4{d[0][0], d[1][0], d[2][0], d[3][0]};
      *(f32x4*)(dst + 132) = f32x4{d[0][2], d[1][2], d[2][2], d[3][2]};
      *(f32x4*)(dst + 4) = f32x4{d[4][0], d[5][0], d[6][0], d[7][0]};
      *(f32x4*)(dst + 136) = f32x4{d[4][2], d[5][2], d[6][2], d[7][2]};
      lds_arrive(&cnt[C_T1], lane);
      lds_wait<0>(&cnt[C_T1], 4);
      {
        const float* src = redC + (2 * cw + (lane >> 5)) * 132 + (lane & 31) * 4;
        const f32x4 s = (*(const f32x4*)src + *(const f32x4*)(src + 1056)) + (*(const f32x4*)(src + 2112) + *(const f32x4*)(src + 3168));
        st_sc(canon4(s), rws, (lane >> 5) * 65536 + (g & 1) * 4096 + n * 16, WS_XP + (cw * 32 + cg * 2) * 4096 + hp * 256);
      }
      if (cw == 0) STAMP(7);
    }
    if (doT3 && cw == 0) {
      lds_wait<8>(&cnt[C_O], 8);
      unsigned t[1];
      gather<PRS>(t, [&](int) { return ld_sc1(rws, n * 256, WS_XP + (xc * 16 + jx) * 4096); }, miss);
      if (PRD) __builtin_amdgcn_s_sleep(PRD);
      lds_arrive(&cnt[C_PRE], lane);
      STAMP(29);
      __builtin_amdgcn_s_setprio(2);
      if (STD) __builtin_amdgcn_s_sleep(STD);
      v4u q[1];
      gather<STS>(q, [&](int) { return ld_sc(rws, lane * 16, WS_X1 + xc * REPX + 16384); }, miss);
      auto s8 = [](float x) { x += dppf<0xB1>(x); x += dppf<0x4E>(x); return x + dppf<0x141>(x); };
      const float m0 = bitsf(q[0].x), m1 = bitsf(q[0].z);
      const float mu = s8(m0 + m1) * (1.f / 16.f);
      const float var = s8(fmaf((m0 - mu) * 64.f, m0 - mu, bitsf(q[0].y)) + fmaf((m1 - mu) * 64.f, m1 - mu, bitsf(q[0].w))) * (1.f / 1024.f);
      *(f32x2*)(mlw + (lane >> 3) * 2) = f32x2{-rbf(mu), rcp(rbf(__builtin_amdgcn_sqrtf(rbf(rbf(var) + 1e-5f))))};
      lds_arrive(&cnt[C_T3], lane);
      STAMP(30);
    }
    if (doT4 && cw == 0) {
      unsigned t[1];
      gather<4>(t, [&](int) { return ld_sc1(rws, (lane & 7) * 2048, WS_X1 + xc * REPX + jb * 128); }, miss);
      if (PRD4) __builtin_amdgcn_s_sleep(PRD4);
      lds_arrive(&cnt[C_PRE], lane);
      STAMP(29);
    }
    if (red) {
      __builtin_amdgcn_s_setprio(2);
      lds_wait<4>(&cnt[C_O], 8);
      if (DIAG == 3) tdiag = now();
      if (DIAG == 5 || DIAG == 6) tdiag = __builtin_amdgcn_readfirstlane(tsx[DIAG]);
      f32x2 x1;
      {
        v2u xp[8];
        gather<1>(xp, [&](int j) { return ld_sc2(rws, half * 2048 + cp * 8, WS_XP + unit * 4096 + j * 256); }, miss);
        const f32x2 s = a32((add2(xp[0], xp[1]) + add2(xp[2], xp[3])) + (add2(xp[4], xp[5]) + add2(xp[6], xp[7])));
        x1 = f32x2{rbf(bflo(xo) + rbf(ga[0] * rbf(s[0]))), rbf(bfhi(xo) + rbf(ga[1] * rbf(s[1])))};
        const unsigned u = canon(pk2(x1[0] * cb1[0], x1[1] * cb1[1]));
#pragma unroll
        for (int r = 0; r < 4; ++r) st_sc1(u, rws, half * REPX + cp * 4, WS_X1 + 2 * r * REPX + xc * 2048 + cblk * 128);
        const float x0 = bitsf(__builtin_amdgcn_readfirstlane(fbits(x1[0])));
        const float e0 = x1[0] - x0, e1 = x1[1] - x0;
        const float sm = sum32(e0 + e1), sq = sum32(fmaf(e0, e0, e1 * e1));
        const float md = sm * (1.f / 64.f);
        if (lane < 8) st_sc2(v2u{canon(fbits(x0 + md)), canon(fbits(fmaxf(sq - sm * md, 0.f)))}, rws, lane * REPX, WS_X1 + 16384 + xc * 128 + cblk * 8);
        if (DIAG == 4) tdiag = now();
        if (cw == 0) STAMP(8);
      }
      __builtin_amdgcn_s_sleep(32);
      {
        v2u dp[4];
        gather<1>(dp, [&](int j) { return j < 3 ? ld_sc2(rws, half * 1024 + cp * 8, WS_DP + unit * 2048 + j * 256) : ld_sc2(rws, half * 768 + cp * 8, WS_DP + unit * 2048 + 768); }, miss);
        if (half) dp[3] = v2u{0u, 0u};
        const f32x2 s = a32(add2(dp[0], dp[1]) + add2(dp[2], dp[3]));
        const unsigned u = pk2(x1[0] + rbf(gb[0] * rbf(s[0])), x1[1] + rbf(gb[1] * rbf(s[1])));
        if (lane < 32) *(unsigned*)(a.out + xc * 1024 + cblk * 64 + cp * 2) = u;
        if (cw == 0) STAMP(25);
        if (DIAG) {
          const unsigned te = now();
          if (DIAG == 1) tdiag = te;
          const unsigned due = AMP * (tdiag - tstart);
#pragma unroll 1
          for (int it = 0; it < 8192; ++it) {
            if ((int)(now() - te) >= (int)due) break;
            __builtin_amdgcn_s_sleep(2);
          }
        }
      }
    }
  }
}

__global__ void mega_end() {}

extern "C" void kernel_launch(void* const* d_in, const int* in_sizes, int n_in, void* d_out, int out_size, void* d_ws, size_t ws_size, hipStream_t stream) {
  Args a;
  a.x = (const u16*)d_in[0]; a.kc = (const u16*)d_in[1]; a.vc = (const u16*)d_in[2]; a.wq = (const u16*)d_in[3]; a.wo = (const u16*)d_in[4];
  a.wg = (const u16*)d_in[6]; a.wu = (const u16*)d_in[7]; a.wd = (const u16*)d_in[8]; a.c = (const u16*)d_in[9]; a.wmod = (const u16*)d_in[10];
  a.out = (u16*)d_out; a.ws = (char*)d_ws;
  mega<<<256, 768, 0, stream>>>(a);
}
```

```cpp
#define DEC 1
#define OSB 1
#define OPD 23
#define G1 450
#define G2 850
#define G3 800
#define PREF 1
#define G4 1200
#define WGW 8
#define G5 2200
#define WE 3
#define SBA 0
#define WE2 3
#define W1 8
#define CKD 2
#define XPB 12
#define PRW4 40
#define PRD 26
#define LAY 4
#define BUR 2
#define ROT 7
#define XPS 90
#define PRW 90
#define PF4 2
#define WDG 1
#define WDW 2
#define WDS 4
#define DPS 100
#define T1F 1
#define HOI 1
#define PA 300
#define TAU 482
#include <hip/hip_runtime.h>
#include <stdint.h>

typedef unsigned short u16;
typedef unsigned long long u64;
typedef unsigned v4u __attribute__((ext_vector_type(4)));
typedef unsigned v2u __attribute__((ext_vector_type(2)));
typedef float f32x4 __attribute__((ext_vector_type(4)));
typedef float f32x2 __attribute__((ext_vector_type(2)));
typedef __bf16 bf16x8 __attribute__((ext_vector_type(8)));
typedef __bf16 bf16x2 __attribute__((ext_vector_type(2)));
typedef __amdgpu_buffer_rsrc_t rsrc_t;
#define DI __device__ __forceinline__
#define SB0() __builtin_amdgcn_sched_barrier(0)
#define CBAR() asm volatile("" ::: "memory")
#define PIN8(f) asm volatile("" : "+v"(f[0]), "+v"(f[1]), "+v"(f[2]), "+v"(f[3]), "+v"(f[4]), "+v"(f[5]), "+v"(f[6]), "+v"(f[7]))
#define VMWAIT(n) asm volatile("s_waitcnt vmcnt(%0)" :: "n"(n) : "memory")

#define PRS 4
#define PRD4 16
#define STD 4
#define STS 2
#define STAMPS 0
#define NP 8
#define NS (NP + 64)
#define QSL 42
#define WIN 8
#define S1 QSL
#define S2 48
#define W3 WIN
#define EXPD 0
#define P0 1
#define ABL 0
#define PS0 0
#define PSD 1
#define WSOFF 0
#define G6 0
#define PK 0
#define PK3 0
#define PXS 6
#define PXD 8
#define PF3 0
#define PFA 0
#define GP 2000
#define SBP 0
#define DC12 1
#define CK0 8
#define XPK 0
#define X1K 0
#define STK 0
#define GUK 0
#define DPK 0
#define OPK 0
#define KF 1
#define LA 1
#define DIAG 0
#define TW 0
#define TV 0
#define TO 0
#define GT 1600
#define PRK 0
#define PR4K 0
#define WDG2 0
#define WDE 0
#define EOS 1
#define PSW 0
#define WR 0
#define PF4S NS
#define TP TAU

#define WT4 0
#define PF5 0
#define PF5P 0
#define GPM 0
#define PF6 0
#define GFS 0
#define STA 16
#define LDA 16
constexpr int WS_EPOCH = 0;
constexpr int WS_FLAGS = 1024, FLAG_REP = 2304;
constexpr int WS_MPA = 32768, MPA_SLOT = 4096 + 256;
constexpr int WS_H = WS_MPA + 256 * MPA_SLOT, HREP = 32768 + 256;
constexpr int WS_ARENA = 2 << 20;
static_assert(WS_H + 8 * HREP <= WS_ARENA, "map");
constexpr int WS_QP = WS_ARENA;
constexpr int WS_OP = WS_QP + (512 << 10);
constexpr int WS_XP = WS_OP + (512 << 10);
constexpr int WS_MPB = WS_XP + (512 << 10);
constexpr int WS_DP = WS_MPB + (256 << 10);
constexpr int REPX = 16384 + 1024 + 256;
constexpr int WS_X1 = WS_DP + (WT4 ? 448 << 10 : 256 << 10);
constexpr int WS_GU = WS_X1 + 8 * REPX;
constexpr int WS_SB = WS_GU + (112 << 10);
constexpr int WS_ML = WS_SB + (32 << 10);
constexpr int WS_END = WS_ML + (16 << 10);
constexpr int ARENA_PER = 10 * 1024;
static_assert(WS_END <= WS_ARENA + 256 * ARENA_PER, "arena");
constexpr int WS_BYTES = 8 << 20;
constexpr unsigned SENT = 0xFFFFFFFFu;
#define CAPSH 0
constexpr int GCAP = 1 << (15 - CAPSH);
constexpr int LCAP = 1 << (22 - CAPSH);

enum { C_RESET = 0, C_GO, C_M1, C_M1S, C_R1, C_Q, C_QS, C_QG, C_MLW, C_MLF, C_M2, C_VI, C_O, C_T1, C_T2, C_PRE, C_C12, C_T3, C_T4, C_PARK, C_PKD, C_CK = 32, C_N = 128 };

DI float bflo(unsigned v) { return __builtin_bit_cast(float, v << 16); }
DI float bfhi(unsigned v) { return __builtin_bit_cast(float, v & 0xffff0000u); }
DI unsigned pk2(float a, float b) { bf16x2 v = {(__bf16)a, (__bf16)b}; unsigned r = __builtin_bit_cast(unsigned, v); asm("" : "+v"(r)); return r; }
DI float rbf(float a) { return (float)(__bf16)a; }
DI unsigned canon(unsigned u) { return u < 0xFFFFFFFEu ? u : 0xFFFFFFFEu; }
DI unsigned fbits(float f) { return __builtin_bit_cast(unsigned, f); }
DI float bitsf(unsigned u) { return __builtin_bit_cast(float, u); }
DI rsrc_t mk(const void* p, unsigned bytes) { return __builtin_amdgcn_make_buffer_rsrc((void*)p, 0, bytes, 0x00020000); }
DI v4u ld_nt(rsrc_t r, int v, int s) { return __builtin_amdgcn_raw_buffer_load_b128(r, v, s, 2); }
DI v4u ld_pl(rsrc_t r, int v, int s) { return __builtin_amdgcn_raw_buffer_load_b128(r, v, s, 0); }
DI unsigned ld_pl1(rsrc_t r, int v, int s) { return __builtin_amdgcn_raw_buffer_load_b32(r, v, s, 0); }
DI v4u ld_sc(rsrc_t r, int v, int s) { return __builtin_amdgcn_raw_buffer_load_b128(r, v, s, LDA); }
DI v2u ld_sc2(rsrc_t r, int v, int s) { return __builtin_amdgcn_raw_buffer_load_b64(r, v, s, LDA); }
DI unsigned ld_sc1(rsrc_t r, int v, int s) { return __builtin_amdgcn_raw_buffer_load_b32(r, v, s, LDA); }
DI void st_sc(v4u x, rsrc_t r, int v, int s) { __builtin_amdgcn_raw_buffer_store_b128(x, r, v, s, STA); }
DI void st_sc2(v2u x, rsrc_t r, int v, int s) { __builtin_amdgcn_raw_buffer_store_b64(x, r, v, s, STA); }
DI void st_sc1(unsigned x, rsrc_t r, int v, int s) { __builtin_amdgcn_raw_buffer_store_b32(x, r, v, s, STA); }
DI f32x4 mfma(v4u a, v4u b, f32x4 c) {
  return __builtin_amdgcn_mfma_f32_16x16x32_bf16(__builtin_bit_cast(bf16x8, a), __builtin_bit_cast(bf16x8, b), c, 0, 0, 0);
}
template <int CTRL> DI unsigned dppu(unsigned x) { return __builtin_amdgcn_update_dpp(0u, x, CTRL, 0xF, 0xF, true); }
template <int CTRL> DI float dppf(float x) { return bitsf(dppu<CTRL>(fbits(x))); }
DI float add8(float x) { return x + dppf<0x128>(x); }
DI float add16(float x) { auto s = __builtin_amdgcn_permlane16_swap(fbits(x), fbits(x), false, false); return bitsf(s[0]) + bitsf(s[1]); }
DI float add32(float x) { auto s = __builtin_amdgcn_permlane32_swap(fbits(x), fbits(x), false, false); return bitsf(s[0]) + bitsf(s[1]); }
DI float max8(float x) { return fmaxf(x, dppf<0x128>(x)); }
DI float max16(float x) { auto s = __builtin_amdgcn_permlane16_swap(fbits(x), fbits(x), false, false); return fmaxf(bitsf(s[0]), bitsf(s[1])); }
DI float max32(float x) { auto s = __builtin_amdgcn_permlane32_swap(fbits(x), fbits(x), false, false); return fmaxf(bitsf(s[0]), bitsf(s[1])); }
DI float row_sum(float x) { x += dppf<0xB1>(x); x += dppf<0x4E>(x); x += dppf<0x141>(x); return x + dppf<0x128>(x); }
DI float sum32(float x) { return add16(row_sum(x)); }
DI float wave_sum(float x) { return add32(sum32(x)); }
DI float ex2(float x) { return __builtin_amdgcn_exp2f(x); }
DI float rcp(float x) { return __builtin_amdgcn_rcpf(x); }
constexpr float LOG2E = 1.4426950408889634f;
DI unsigned pair_pack(float v) { return dppu<0xA0>(pk2(v, dppf<0xB1>(v))); }

typedef __attribute__((address_space(3))) unsigned lunsigned;
DI void lds_arrive(unsigned cb, int i, unsigned one = 1u) {
  u64 keep;
  asm volatile("s_mov_b64 %0, exec\n\ts_mov_b64 exec, 1\n\tds_add_u32 %1, %2 offset:%3\n\ts_mov_b64 exec, %0" : "=&s"(keep) : "v"(cb), "v"(one), "n"(i * 4) : "memory");
}
template <int SLEEP> DI void lds_wait(unsigned cb, int c, unsigned target) {
  unsigned v, i = LCAP;
  if (SLEEP)
    asm volatile(".Lw%=:\n\tds_read_b32 %0, %2 offset:%5\n\ts_waitcnt lgkmcnt(0)\n\tv_cmp_gt_u32 vcc, %3, %0\n\ts_cbranch_vccz .Le%=\n\ts_sleep %4\n\ts_sub_u32 %1, %1, 1\n\ts_cbranch_scc0 .Lw%=\n.Le%=:"
                 : "=&v"(v), "+s"(i) : "v"(cb), "n"(target), "n"(SLEEP), "n"(c * 4) : "memory", "vcc", "scc");
  else
    asm volatile(".Lw%=:\n\tds_read_b32 %0, %2 offset:%4\n\ts_waitcnt lgkmcnt(0)\n\tv_cmp_gt_u32 vcc, %3, %0\n\ts_cbranch_vccz .Le%=\n\ts_sub_u32 %1, %1, 1\n\ts_cbranch_scc0 .Lw%=\n.Le%=:"
                 : "=&v"(v), "+s"(i) : "v"(cb), "n"(target), "n"(c * 4) : "memory", "vcc", "scc");
}
DI unsigned umax4(v4u x) { return max(max(x.x, x.y), max(x.z, x.w)); }
template <int N, class T, class F> DI void gat_issue(T (&v)[N], F f) {
  CBAR();
#pragma unroll
  for (int n = 0; n < N; ++n) v[n] = f(n);
  CBAR();
}
template <int SLEEP, int N, class T, class F, class M> DI void gat_finish(T (&v)[N], F f, M miss) {
  CBAR();
  if (ABL & 4) return;
  bool m[N], any = false;
#pragma unroll
  for (int n = 0; n < N; ++n) { m[n] = miss(v[n]); any |= m[n]; }
  if (__builtin_amdgcn_ballot_w64(any) != 0) {
#pragma unroll 1
    for (int i = 0; i < GCAP; ++i) {
      if (SLEEP) __builtin_amdgcn_s_sleep(SLEEP);
      CBAR();
#pragma unroll
      for (int n = 0; n < N; ++n) if (m[n]) v[n] = f(n);
      CBAR();
      any = false;
#pragma unroll
      for (int n = 0; n < N; ++n) { m[n] = miss(v[n]); any |= m[n]; }
      if (__builtin_amdgcn_ballot_w64(any) == 0) break;
    }
  }
  CBAR();
}
template <int SLEEP, int N, class T, class F, class M> DI void gather(T (&v)[N], F f, M miss) { gat_issue(v, f); gat_finish<SLEEP>(v, f, miss); }
DI void ov_ld(v4u& v, int voff, v4u srd, int soff) { asm volatile("buffer_load_dwordx4 %0, %1, %2, %3 offen sc1" : "+v"(v) : "v"(voff), "s"(srd), "s"(soff) : "memory"); }
DI void ov_ld(v2u& v, int voff, v4u srd, int soff) { asm volatile("buffer_load_dwordx2 %0, %1, %2, %3 offen sc1" : "+v"(v) : "v"(voff), "s"(srd), "s"(soff) : "memory"); }
DI void ov_ld(unsigned& v, int voff, v4u srd, int soff) { asm volatile("buffer_load_dword %0, %1, %2, %3 offen sc1" : "+v"(v) : "v"(voff), "s"(srd), "s"(soff) : "memory"); }
DI unsigned umx(unsigned m, unsigned x) { return max(m, x); }
DI unsigned umx(unsigned m, v4u x) { return max(max(max(m, x.x), x.y), max(x.z, x.w)); }
DI unsigned umx(unsigned m, v2u x) { return max(max(m, x.x), x.y); }
template <int K, int N, class T, class F, class M, class G> DI void ov_gather(T (&v)[N], F f, M miss, G mid) {
#pragma unroll
  for (int n = 0; n < N; ++n) { v[n] = SENT; f(v[n], n); }
  mid();
  int left = K;
#pragma unroll 1
  for (int i = 0; i < GCAP * 8; ++i) {
    __builtin_amdgcn_s_sleep(1);
    unsigned mx = 0;
#pragma unroll
    for (int n = 0; n < N; ++n) { asm volatile("" : "+v"(v[n])); mx = umx(mx, v[n]); }
    if (__builtin_amdgcn_ballot_w64(mx == SENT) == 0) break;
    if (--left == 0) {
      left = K;
      VMWAIT(62 - N);
#pragma unroll
      for (int n = 0; n < N; ++n) { asm volatile("" : "+v"(v[n])); if (miss(v[n])) f(v[n], n); }
    }
  }
  CBAR();
}
template <int N, class T> DI void ov_keep(T (&v)[N]) {
  VMWAIT(0);
#pragma unroll
  for (int n = 0; n < N; ++n) asm volatile("" :: "v"(v[n]));
}
typedef __attribute__((address_space(3))) float lfloat;
DI unsigned ldsa(const float* p) { return (unsigned)(uintptr_t)(const lfloat*)p; }
DI void dma_nt(unsigned lds, int voff, v4u srd, int soff) {
  unsigned keep;
  asm volatile("s_mov_b32 %0, m0\n\ts_mov_b32 m0, %1\n\ts_nop 0\n\tbuffer_load_dwordx4 %2, %3, %4 offen nt lds\n\ts_mov_b32 m0, %0"
               : "=&s"(keep) : "s"(lds), "v"(voff), "s"(srd), "s"(soff) : "memory");
}
DI unsigned now() { return (unsigned)__builtin_amdgcn_s_memrealtime(); }
DI void pace(unsigned t0, unsigned due) {
#pragma unroll 1
  for (int it = 0; it < 2048; ++it) {
    if ((int)(now() - t0) >= (int)due) break;
    __builtin_amdgcn_s_sleep(1);
  }
}
struct Miss {
  DI bool operator()(v4u x) const { return umax4(x) == SENT; }
  DI bool operator()(v2u x) const { return max(x.x, x.y) == SENT; }
  DI bool operator()(unsigned x) const { return x == SENT; }
};
DI unsigned um3(unsigned m, unsigned x) { return max(m, x); }
DI unsigned um3(unsigned m, v2u x) { return max(max(m, x.x), x.y); }
DI unsigned um3(unsigned m, v4u x) { return max(max(max(max(m, x.x), x.y), x.z), x.w); }
template <int SLEEP, int N, class T, class F> DI void fin(T (&v)[N], F f) {
  const Miss miss;
  if (!GFS) { gat_finish<SLEEP>(v, f, miss); return; }
  CBAR();
#pragma unroll 1
  for (int i = 0; i < GCAP; ++i) {
    unsigned mx = 0;
#pragma unroll
    for (int n = 0; n < N; ++n) mx = um3(mx, v[n]);
    if (__builtin_amdgcn_ballot_w64(mx == SENT) == 0) break;
    if (SLEEP) __builtin_amdgcn_s_sleep(SLEEP);
    CBAR();
#pragma unroll
    for (int n = 0; n < N; ++n) { asm volatile("" : "+v"(v[n])); if (miss(v[n])) v[n] = f(n); }
    CBAR();
  }
  CBAR();
}
template <int SLEEP, int N, class T, class F, class M> DI void gatherS(T (&v)[N], F f, M) { gat_issue(v, f); fin<SLEEP>(v, f); }
#define PINV(x) do { if (HOI) asm volatile("" : "+v"(x)); } while (0)
#define PINS(x) do { if (HOI) asm volatile("" : "+s"(x)); } while (0)
DI void tr8(const v4u (&w)[8], v4u (&f)[8]) {
#pragma unroll
  for (int m = 0; m < 4; ++m)
#pragma unroll
    for (int p = 0; p < 4; ++p) {
      f[2 * m][p] = __builtin_amdgcn_perm(w[2 * p + 1][m], w[2 * p][m], 0x05040100u);
      f[2 * m + 1][p] = __builtin_amdgcn_perm(w[2 * p + 1][m], w[2 * p][m], 0x07060302u);
    }
}
DI void kfold_store(const f32x4 (&d)[8], float* slot, int lane) {
  const bool lo = (lane & 8) == 0;
  unsigned mk = lo ? ~0u : 0u;
  asm("" : "+v"(mk));
  float o[8];
#pragma unroll
  for (int j = 0; j < 8; ++j) {
    float x, y;
    if (KF) { x = bitsf((fbits(d[j][0]) & mk) | (fbits(d[j][3]) & ~mk)); y = bitsf((fbits(d[j][2]) & mk) | (fbits(d[j][1]) & ~mk)); asm("" : "+v"(x)); }
    else { x = lo ? d[j][0] : d[j][3]; y = lo ? d[j][2] : d[j][1]; }
    o[j] = x + dppf<0x128>(y);
    if (KF) asm("" : "+v"(o[j]));
  }
  *(f32x4*)(slot + lane * 8) = f32x4{o[0], o[1], o[2], o[3]};
  *(f32x4*)(slot + lane * 8 + 4) = f32x4{o[4], o[5], o[6], o[7]};
}
DI void mm_fold(f32x4 (&d)[8], v4u a, const v4u (&b)[8], unsigned mk, unsigned at) {
  typedef __attribute__((address_space(3))) f32x4 lf4;
  float x[8], y[8];
  SB0();
#pragma unroll
  for (int j = 0; j < 12; ++j) {
    if (j < 8) d[j] = mfma(a, b[j], d[j]);
    SB0();
    if (j >= 3 && j < 11) { x[j - 3] += dppf<0x128>(y[j - 3]); asm("" : "+v"(x[j - 3])); }
    if (j >= 2 && j < 10) {
      const f32x4 e = d[j - 2];
      y[j - 2] = bitsf((fbits(e[2]) & mk) | (fbits(e[1]) & ~mk)); x[j - 2] = bitsf((fbits(e[0]) & mk) | (fbits(e[3]) & ~mk));
      asm("" : "+v"(x[j - 2]), "+v"(y[j - 2]));
    }
    if (j == 6) *(lf4*)(uintptr_t)at = f32x4{x[0], x[1], x[2], x[3]};
    if (j == 10) *(lf4*)(uintptr_t)(at + 16) = f32x4{x[4], x[5], x[6], x[7]};
    SB0();
  }
}
DI float sum8l(unsigned at, int stride) {
  float v[8];
#pragma unroll
  for (int i = 0; i < 8; ++i) v[i] = *(const lfloat*)(uintptr_t)(at + i * stride * 4);
  SB0();
  auto A = [](float x, float y) { x += y; asm("" : "+v"(x)); return x; };
  return A(A(A(v[0], v[1]), A(v[2], v[3])), A(A(v[4], v[5]), A(v[6], v[7])));
}
DI float sum8(const float* p, int stride) {
  return ((p[0] + p[stride]) + (p[2 * stride] + p[3 * stride])) + ((p[4 * stride] + p[5 * stride]) + (p[6 * stride] + p[7 * stride]));
}
DI f32x2 sum8x2(const float* p, int stride) {
  auto L = [&](int i) { return *(const f32x2*)(p + i * stride); };
  return ((L(0) + L(1)) + (L(2) + L(3))) + ((L(4) + L(5)) + (L(6) + L(7)));
}
DI v4u canon4(f32x4 s) { return v4u{canon(fbits(s[0])), canon(fbits(s[1])), canon(fbits(s[2])), canon(fbits(s[3]))}; }
DI f32x4 asf(v4u x) { return __builtin_bit_cast(f32x4, x); }
DI v4u silu8(v4u c) {
  v4u r;
#pragma unroll
  for (int m = 0; m < 4; ++m) {
    float a = bflo(c[m]), b = bfhi(c[m]);
    a = a * rcp(1.f + ex2(-a * LOG2E)); b = b * rcp(1.f + ex2(-b * LOG2E));
    r[m] = pk2(a, b);
  }
  return r;
}

struct Args {
  const u16 *x, *kc, *vc, *wq, *wo, *wg, *wu, *wd, *c, *wmod;
  u16* out;
  char* ws;
};

#define STAMPW(i) do { } while (0)
#define STAMP(i) do { } while (0)

template <int I> struct IC { static constexpr int v = I; };
template <int I, int N, class F> DI void sfor(F&& f) { if constexpr (I < N) { f(IC<I>{}); sfor<I + 1, N>(f); } }
constexpr int imax(int a, int b) { return a > b ? a : b; }
constexpr int win(int i) { return i < S1 ? W1 : (i < S2 ? WIN : W3); }
constexpr int landed(int j) { int i = j + 1; while (i < NS && i - win(i) < j) ++i; return i; }
constexpr int pidx(int i) { return (i >= PS0 && (i - PS0) % PSD == 0 && (i - PS0) / PSD < NP) ? (i - PS0) / PSD : -1; }
constexpr int kidx(int i) { int c = 0; for (int s = 0; s < i; ++s) c += pidx(s) < 0; return c; }
constexpr int kslot(int j) { int i = 0; while (pidx(i) >= 0 || kidx(i) != j) ++i; return i; }
static_assert(PS0 + PSD * (NP - 1) < NP + 32, "parked loads");
constexpr int KL0 = landed(kslot(3)), KL1 = landed(kslot(7)), KL2 = landed(kslot(11)), KL3 = landed(kslot(15)), KL4 = landed(kslot(19)), KL5 = landed(kslot(23)), KL6 = landed(kslot(27)), KL7 = landed(kslot(31));
constexpr int ktr_slot(int t, int m) { return (t == 0 ? KL0 : t == 1 ? KL1 : t == 2 ? KL2 : t == 3 ? KL3 : t == 4 ? KL4 : t == 5 ? KL5 : t == 6 ? KL6 : KL7) + (t < 7 ? m : m / 2); }
constexpr int qk_slot(int t) { return imax(ktr_slot(t, 3) + (t < 7 ? 1 : 0), QSL + t / 2); }
constexpr int EXP0 = qk_slot(7) + 1 + EXPD;
constexpr int PV0 = EXP0 + 3;
constexpr int imin(int a, int b) { return a < b ? a : b; }
constexpr int pv_slot(int blk, int m) { return imin(imax(PV0 + 2 * blk, landed(NP + 39 + 8 * blk)) + m / 2, NS); }
constexpr int RW = NP * 256;
static_assert(PXS + PXD * (PK3 - 1) < NS - 8 && PK <= PK3 && RW >= 1536 && PV0 <= NS && landed(NP + 31) <= S2, "schedule");

typedef unsigned v16u __attribute__((ext_vector_type(16)));
typedef unsigned v8u __attribute__((ext_vector_type(8)));
__global__ void __launch_bounds__(768, 3) mega(Args) {
  __shared__ __attribute__((aligned(16))) unsigned cnt[C_N];
  __shared__ __attribute__((aligned(16))) unsigned qs[8 * 68];
  __shared__ __attribute__((aligned(16))) float redO[8 * 1056];
  __shared__ __attribute__((aligned(16))) float redC[4 * 1056];
  __shared__ __attribute__((aligned(16))) float mlw[8 * 16];
  __shared__ __attribute__((aligned(16))) float mlf[16];
  __shared__ __attribute__((aligned(1024))) float reg[8 * RW];
  __shared__ __attribute__((aligned(1024))) float pre[8 * PK3 * 256 + 4];
  float* const redT = reg;
  float* const redU = reg + 1024;

  v16u ka0;
  v8u ka1;
  asm volatile("s_load_dwordx16 %0, %2, 0x0\n\ts_load_dwordx8 %1, %2, 0x40" : "=&s"(ka0), "=&s"(ka1) : "s"(__builtin_amdgcn_kernarg_segment_ptr()));
  unsigned pfc = 0;
  if (PREF == 2) {
    u64 pc;
    unsigned len;
    asm volatile("s_getpc_b64 %0\n.Lref%=:\n\ts_mov_b32 %1, .Lref%=-_Z4mega4Args" : "=s"(pc), "=s"(len));
    pc -= len;
    asm volatile("s_mov_b32 %0, _Z8mega_endv-_Z4mega4Args" : "=s"(len));
    pfc = ld_pl1(mk((const void*)pc, len), threadIdx.x * 64, 0);
    SB0();
  }
  const int bid = blockIdx.x, lane = threadIdx.x & 63;
  const int w = __builtin_amdgcn_readfirstlane(threadIdx.x >> 6);
  const int xc = bid & 7, jx = bid >> 3;
  const int ab = xc, akvh = jx >> 3, as = jx & 7;
  const int grp = ab * 4 + akvh;
  const bool typeB = jx >= 16;
  const int jb = jx - 16;
  const int g = lane >> 4, n = lane & 15;
  const int arow = (n >> 1) * 2048 + (n & 1) * 64 + g * 16;
  const bool doT3 = jx < 14, doT4 = typeB && jb < 14;
  const Miss miss;
  unsigned cb = (unsigned)(uintptr_t)(lunsigned*)cnt;
  asm("" : "+s"(cb));
  if (threadIdx.x < C_N) cnt[threadIdx.x] = 0;
  const unsigned tstart = now();
  SB0();
  asm volatile("s_waitcnt lgkmcnt(0)" : "+s"(ka0), "+s"(ka1));
  Args a;
  {
    auto P = [](unsigned lo, unsigned hi) { return (u16*)(((u64)hi << 32) | lo); };
    a.x = P(ka0[0], ka0[1]); a.kc = P(ka0[2], ka0[3]); a.vc = P(ka0[4], ka0[5]); a.wq = P(ka0[6], ka0[7]);
    a.wo = P(ka0[8], ka0[9]); a.wg = P(ka0[10], ka0[11]); a.wu = P(ka0[12], ka0[13]); a.wd = P(ka0[14], ka0[15]);
    a.c = P(ka1[0], ka1[1]); a.wmod = P(ka1[2], ka1[3]); a.out = P(ka1[4], ka1[5]); a.ws = (char*)P(ka1[6], ka1[7]);
  }
  const rsrc_t rws = mk(a.ws, WS_BYTES);
  const v4u sws = {ka1[6], ka1[7] & 0xffffu, (unsigned)WS_BYTES, 0x00020000u};
  auto nop = []() {};
  const int ta = DEC ? (bid % 14) * 8 + ((bid / 14 + 2) & 7) : xc * 14 + jx;
  const int m2c = DEC ? ((jx + 5) & 7) + 8 * xc : bid & 63;
  const int wl = (n >> 3) * 32 + g * 8, wc = (n & 7) * 16;

  if (w < 8) {
    if (w == 0) STAMP(0);
    {
      const v4u S = {SENT, SENT, SENT, SENT};
      if (!(ABL & 1)) st_sc(S, rws, lane * 16, WS_ARENA + bid * ARENA_PER + w * 1024);
      if (!(ABL & 1)) st_sc(S, rws, lane * 16, WS_ARENA + bid * ARENA_PER + (w < 2 ? 8 + w : w) * 1024);
    }
    const rsrc_t rk = mk(a.kc, 64u << 20), rv = mk(a.vc, 64u << 20);
    const int wa = (w + (WR == 1 ? ab : WR == 2 ? akvh * 2 : WR == 3 ? ab + akvh * 2 : 0)) & 7;
    const int kvbase = LAY == 6 ? grp * (2 << 20) + as * 1024 + wa * 16384 : LAY == 4 ? grp * (2 << 20) + as * 1024 + wa * 8192 : (grp * 8 + as) * (256 << 10) + w * 1024;
    const int koff = kvbase + ((lane >> 2) & 3) * 256 + g * 64 + (lane & 3) * 16;
    const int voff = kvbase + g * 256 + n * 16;
    const bool l0 = lane & 1, l1 = lane & 2;
    const int td = xc * 14 + jb;
    v4u k[32], v[32], wt[16], qf[4], P[4];
    f32x4 S[8], O[8];
    float mw = -INFINITY, lw = 0.f, nm = 0.f;
    auto ktr = [&](int t, int m) {
      unsigned r0 = k[4 * t][m], r1 = k[4 * t + 1][m], r2 = k[4 * t + 2][m], r3 = k[4 * t + 3][m];
      const unsigned d0 = dppu<0xB1>(r0), d1 = dppu<0xB1>(r1), d2 = dppu<0xB1>(r2), d3 = dppu<0xB1>(r3);
      const unsigned s0 = l0 ? d1 : r0, s1 = l0 ? r1 : d0, s2 = l0 ? d3 : r2, s3 = l0 ? r3 : d2;
      const unsigned e0 = dppu<0x4E>(s0), e1 = dppu<0x4E>(s1), e2 = dppu<0x4E>(s2), e3 = dppu<0x4E>(s3);
      k[4 * t][m] = l1 ? e2 : s0; k[4 * t + 2][m] = l1 ? s2 : e0;
      k[4 * t + 1][m] = l1 ? e3 : s1; k[4 * t + 3][m] = l1 ? s3 : e1;
      asm volatile("" : "+v"(k[4 * t]), "+v"(k[4 * t + 1]), "+v"(k[4 * t + 2]), "+v"(k[4 * t + 3]));
      SB0();
    };
    auto qk = [&](int t) {
      f32x4 s = {0.f, 0.f, 0.f, 0.f};
#pragma unroll
      for (int i = 0; i < 4; ++i) s = mfma(k[4 * t + i], qf[i], s);
      unsigned p0 = pk2(s[0], s[1]), p1 = pk2(s[2], s[3]);
      const float sc = 0.08838834764831845f;
      p0 = pk2(bflo(p0) * sc, bfhi(p0) * sc); p1 = pk2(bflo(p1) * sc, bfhi(p1) * sc);
      S[t] = f32x4{bflo(p0), bfhi(p0), bflo(p1), bfhi(p1)};
      mw = fmaxf(mw, fmaxf(fmaxf(S[t][0], S[t][1]), fmaxf(S[t][2], S[t][3])));
      asm volatile("" : "+v"(S[t]), "+v"(mw));
    };
    auto expo = [&](int t) {
      float e0 = ex2(fmaf(S[t][0], LOG2E, nm)), e1 = ex2(fmaf(S[t][1], LOG2E, nm)), e2 = ex2(fmaf(S[t][2], LOG2E, nm)), e3 = ex2(fmaf(S[t][3], LOG2E, nm));
      lw += (e0 + e1) + (e2 + e3);
      unsigned p0 = pk2(e0, e1), p1 = pk2(e2, e3);
      asm volatile("" : "+v"(p0), "+v"(p1), "+v"(lw));
      P[t >> 1][(t & 1) * 2] = p0; P[t >> 1][(t & 1) * 2 + 1] = p1;
    };
    auto pv = [&](int blk, int m) {
      v4u f0, f1;
#pragma unroll
      for (int p = 0; p < 4; ++p) {
        f0[p] = __builtin_amdgcn_perm(v[8 * blk + 2 * p + 1][m], v[8 * blk + 2 * p][m], 0x05040100u);
        f1[p] = __builtin_amdgcn_perm(v[8 * blk + 2 * p + 1][m], v[8 * blk + 2 * p][m], 0x07060302u);
      }
      const f32x4 z = {0.f, 0.f, 0.f, 0.f};
      O[2 * m] = mfma(f0, P[blk], blk ? O[2 * m] : z);
      O[2 * m + 1] = mfma(f1, P[blk], blk ? O[2 * m + 1] : z);
      asm volatile("" : "+v"(O[2 * m]), "+v"(O[2 * m + 1]));
      SB0();
    };
    SB0();
    const int rj = ROT == 4 ? grp : ROT == 5 ? grp * 11 : ROT == 6 ? akvh * 8 : ROT == 7 ? ab * 4 : ROT == 8 ? ab * 4 + (akvh >> 1) * 2 : ROT == 9 ? ab * 12 : ROT == 10 ? ab * 2 + (akvh & 1) * 16 : 0;
    auto pos = [&](int j) { return LAY == 6 ? ((j + rj) & 30) * 65536 + (j & 1) * 8192 : LAY == 4 ? ((j + rj) & 31) * 65536 : j * 8192; };
    const u64 pb0 = typeB ? (u64)a.wo : (u64)a.wmod, pb1 = ta >= 56 ? (u64)a.wu : (u64)a.wg;
    const v4u srd0 = {(unsigned)pb0, (unsigned)(pb0 >> 32) & 0xffffu, typeB ? 8u << 20 : 12u << 20, 0x00020000u};
    auto ps0 = [&](int i) { return typeB ? (EOS ? (i & 3) * 8 + (i >> 2) : i) * 2048 : i * 12288; };
    const int pv0 = typeB ? ((2 * xc + (jb >> 3)) * 256 + w * 32 + g * (EOS ? 2 : 8)) * 2048 + (jb & 7) * 256 + n * 16 : ((jx >> 3) * 512 + w * 64 + wl) * 12288 + 4096 + m2c * 128 + wc;
    const int pv1 = (w * 128 + (GPM ? wl >> 2 : wl)) * 7168 + (ta % 56) * 128 + wc;
    auto wl_off = [](int i) { return ((i >> 3) * 64 + (GPM ? (i & 6) * 8 + (i & 1) : i & 7)) * 7168; };
    const unsigned park = ldsa(reg + w * RW), park2 = ldsa(pre + w * (PK3 * 256));
    const unsigned parkx = ldsa(reg) + w * 1024;
    const int psx = typeB ? 32 * 2048 : 64 * 12288, pv0x = pv0 - w * psx + ps0(w);
    const bool slow = (ta & 7) == 3;
    const v4u srd1 = {(unsigned)pb1, (unsigned)(pb1 >> 32) & 0xffffu, doT3 ? 7u << 20 : 0u, 0x00020000u};
    const v4u srd2 = {(unsigned)pb1, (unsigned)(pb1 >> 32) & 0xffffu, doT3 && slow ? 7u << 20 : 0u, 0x00020000u};
    const rsrc_t rwl = mk((const void*)pb1, doT3 && !(ABL & 8) ? 7u << 20 : 0u);
    const int ob = jb < 8 ? 16 + xc : (xc >= 6 ? 8 + xc : 24 + xc), ot = (ob % 14) * 8 + 3;
    const rsrc_t rp4 = mk(ot >= 56 ? a.wu : a.wg, typeB && ob < 28 ? 7u << 20 : 0u);
    unsigned pf4 = 0, pf5 = 0, pf6 = 0;
    const int o5 = PF6 ? (jx == 14 ? 16 + xc : (xc >= 6 ? 8 + xc : 24 + xc)) : jb * 8 + xc, t5 = (o5 % 14) * 8 + ((o5 / 14 + 2) & 7);
    const rsrc_t rp5 = mk(t5 >= 56 ? a.wu : a.wg, (PF6 ? (jx & 30) == 14 && o5 < 28 : doT4 && (t5 & 7) != 3) ? 7u << 20 : 0u);
    auto pf5_ld = [&]() {
      if (PF5 == 3) { if ((lane & 7) >= 4) pf5 = ld_pl1(rp5, lane * 7168, (w * 128 + 64) * 7168 + (t5 % 56) * 128); }
      else pf5 = ld_pl1(rp5, lane * 7168, (w * 128 + 64) * 7168 + (t5 % 56) * 128);
      if (PF5 == 2) pf6 = ld_pl1(rp5, lane * 7168, (w * 128) * 7168 + (t5 % 56) * 128);
      if (PF5 == 4) { if ((lane & 7) >= 4) pf6 = ld_pl1(rp5, lane * 7168, (w * 128) * 7168 + (t5 % 56) * 128); }
      SB0();
    };
    const unsigned tw = tstart + (w * BUR * TAU) / 128;
    unsigned tnow = tw, ckv = 0;
    sfor<0, NS + 1>([&](auto ic) __attribute__((always_inline)) {
      constexpr int i = decltype(ic)::v;
      if constexpr (i >= W1 && i < NS) VMWAIT(win(i) - 1);
      if constexpr (TAU && i >= P0 && i < NS && (i % BUR == 0 || i == P0)) {
        constexpr int ib = (i / BUR) * BUR;
        constexpr unsigned due = PA + (imin(ib, NP) * TP + imax(ib - NP, 0) * TAU) / 16;
        if ((int)(tnow - tw) < (int)due) pace(tw, due);
      }
      if constexpr (CKD && i >= CK0 + CKD && i < NS && (i - CK0) % CKD == 0) {
        if (__builtin_amdgcn_readfirstlane(ckv) < 8) lds_wait<1>(cb, C_CK + (i - CK0) / CKD - 1, 8);
      }
      SB0();
      constexpr int pi = pidx(i);
      if constexpr (pi >= 0) { if (PSW) dma_nt(parkx + pi * RW * 4, pv0x, srd0, pi * psx); else dma_nt(park + pi * 1024, pv0, srd0, ps0(pi)); }
      else if constexpr (i < NP + 32) { constexpr int ki = kidx(i); k[ki] = ld_nt(rk, koff, pos(ki)); }
      else if constexpr (i < NS) v[i - NP - 32] = ld_nt(rv, voff, pos(i - NP - 32));
      if constexpr (PF4 && i == PF4S) pf4 = ld_pl1(rp4, (PF4 == 1 ? 64 + w * 8 + (lane & 7) : PF4 == 2 ? w * 16 + n : (lane & 8 ? (lane & 4) * 8 + (lane & 2) * 8 + (w & 1) * 8 + (w >> 1) + 4 * (lane & 1) : 64 + w * 8 + (lane & 7))) * 7168, (jb & 7) * 128 * 7168 + (ot % 56) * 128);
      if constexpr (PF5 && PF5P == 0 && i == NS) pf5_ld();
      if constexpr (i >= PXS && (i - PXS) % PXD == 0 && (i - PXS) / PXD < PK3) {
        constexpr int p = PK3 - 1 - (i - PXS) / PXD;
        dma_nt(park2 + p * 1024, pv1, p < PK ? srd1 : srd2, wl_off(p));
      }
      SB0();
      if constexpr (CKD && i >= CK0 && i + CKD < NS && (i - CK0) % CKD == 0) lds_arrive(cb, C_CK + (i - CK0) / CKD);
      if constexpr (CKD && i + 1 >= CK0 + CKD && i + 1 < NS && (i + 1 - CK0) % CKD == 0)
        ckv = __hip_atomic_load(&cnt[C_CK + (i + 1 - CK0) / CKD - 1], __ATOMIC_RELAXED, __HIP_MEMORY_SCOPE_WORKGROUP);
      if constexpr (TAU && i + 1 >= P0 && i + 1 < NS && ((i + 1) % BUR == 0 || i + 1 == P0)) { tnow = now(); SB0(); }
      if constexpr (i == P0 - 1) {
        asm volatile("s_waitcnt lgkmcnt(0)\n\ts_barrier\n\ts_waitcnt vmcnt(%0)" :: "n"(P0) : "memory");
        asm volatile("" :: "v"(pfc));
        lds_arrive(cb, C_RESET);
        SB0();
      }
      if constexpr (i == landed(PS0 + PSD * (NP - 1))) lds_arrive(cb, C_PARK);
      if constexpr (i % 12 == 3) STAMPW(i / 12);
      SB0();
#pragma unroll
      for (int t = 0; t < 8; ++t)
#pragma unroll
        for (int m = 0; m < 4; ++m)
          if (ktr_slot(t, m) == i) ktr(t, m);
      if constexpr (i == QSL) {
        if (w == 0) STAMP(1);
        lds_wait<2>(cb, C_QG, 4);
        if (w == 0) STAMP(2);
#pragma unroll
        for (int j = 0; j < 4; ++j) qf[j] = *(const v4u*)(qs + (n & 7) * 68 + g * 16 + j * 4);
      }
#pragma unroll
      for (int t = 0; t < 8; ++t)
        if (qk_slot(t) == i) qk(t);
      if constexpr (i == EXP0 - 1) { mw = max32(max16(mw)); nm = -mw * LOG2E; }
      if constexpr (i >= EXP0 && i < EXP0 + 3) { expo(3 * (i - EXP0)); expo(3 * (i - EXP0) + 1); if constexpr (i < EXP0 + 2) expo(3 * (i - EXP0) + 2); }
      if constexpr (i == EXP0 + 2) {
        lw = add32(add16(lw));
        if (lane < 8) *(f32x2*)(mlw + w * 16 + lane * 2) = f32x2{mw, lw};
        CBAR();
        unsigned old = 0;
        if (lane == 0) old = __hip_atomic_fetch_add(&cnt[C_MLW], 1u, __ATOMIC_RELAXED, __HIP_MEMORY_SCOPE_WORKGROUP);
        old = __builtin_amdgcn_readfirstlane(old);
        CBAR();
        if (old == 7) {
          f32x2 r = *(const f32x2*)(mlw + lane * 2);
          float M = max32(max16(max8(r[0])));
          float L = add32(add16(add8(r[1] * ex2((r[0] - M) * LOG2E))));
          if (lane < 8) {
            *(f32x2*)(mlf + lane * 2) = f32x2{M, L};
            st_sc2(v2u{canon(fbits(M)), canon(fbits(L))}, rws, lane * 512, WS_ML + akvh * 4096 + ab * 64 + as * 8);
          }
          lds_arrive(cb, C_MLF);
        }
        if (w == 0) STAMP(3);
      }
#pragma unroll
      for (int blk = 0; blk < 3; ++blk)
#pragma unroll
        for (int m = 0; m < 4; ++m)
          if (pv_slot(blk, m) == i) pv(blk, m);
      SB0();
    });
    float fs, fw[8];
    {
      lds_wait<1>(cb, C_MLF, 1);
      if (EOS) {
        const f32x2 ml = *(const f32x2*)(mlf + w * 2);
        const float rl = rcp(ml[1]);
#pragma unroll
        for (int i = 0; i < 8; ++i) fw[i] = ex2((mlw[i * 16 + w * 2] - ml[0]) * LOG2E) * rl;
        PIN8(fw);
      } else {
        f32x2 ml = *(const f32x2*)(mlf + (n & 7) * 2);
        fs = ex2((mw - ml[0]) * LOG2E) * rcp(ml[1]);
        asm volatile("" : "+v"(fs));
      }
    }
    SB0();
    typedef __attribute__((address_space(3))) f32x4 lf4;
    typedef __attribute__((address_space(3))) f32x2 lf2;
    unsigned cv = cb, one = 1u;
    if (HOI > 1) asm volatile("" : "+v"(cv), "+v"(one));
    unsigned ow = ldsa(redO + w * 1056 + (n & 7) * 132 + g * 32), ord = ldsa(redO + w * 132 + lane * 2);
    int opv = g * 4096 + n * 4, ops = WS_OP + (akvh * 8 + w) * 16384 + (as >> 1) * 1024 + ab * 128 + (as & 1) * 64;
    PINV(ow); PINV(ord); PINV(opv); PINS(ops);
    SB0();
    v4u sbcb[4];
    const rsrc_t rsb = mk(a.ws, doT3 ? WS_BYTES : 0);
    auto aux_off = [&](int i) { return WS_SB + w * 256 + (i >> 1) * 16384 + (i & 1) * 128; };
    auto aux_first = [&](int i) { return __builtin_amdgcn_raw_buffer_load_b128(rsb, arow, aux_off(i), SBA); };
    if (SBP == 2) gat_issue(sbcb, aux_first);
    SB0();
#pragma unroll
    for (int i = 0; i < WE; ++i) wt[PK3 + i] = ld_nt(rwl, pv1, wl_off(PK3 + i));
    SB0();
    if (EOS) {
      v4u fv[8];
#pragma unroll
      for (int p = 0; p < 4; ++p) {
#pragma unroll
        for (int m = 0; m < 4; ++m) {
          fv[2 * m][p] = __builtin_amdgcn_perm(v[24 + 2 * p + 1][m], v[24 + 2 * p][m], 0x05040100u);
          fv[2 * m + 1][p] = __builtin_amdgcn_perm(v[24 + 2 * p + 1][m], v[24 + 2 * p][m], 0x07060302u);
          if (p == 3 && HOI < 2) { O[2 * m] = mfma(fv[2 * m], P[3], O[2 * m]); O[2 * m + 1] = mfma(fv[2 * m + 1], P[3], O[2 * m + 1]); }
        }
        SB0();
      }
      if (HOI > 1) {
#pragma unroll
        for (int j = 0; j < 10; ++j) {
          if (j < 8) O[j] = mfma(fv[j], P[3], O[j]);
          SB0();
          if (j >= 2) *(lf4*)(uintptr_t)(ow + (j - 2) * 16) = O[j - 2];
          SB0();
        }
      } else if (n < 8) {
#pragma unroll
        for (int j = 0; j < 8; ++j) *(lf4*)(uintptr_t)(ow + j * 16) = O[j];
      }
      lds_arrive(cv, C_O, one);
    } else {
#pragma unroll
      for (int m = 0; m < 4; ++m) pv(3, m);
    }
    if (SBP == 1) gat_issue(sbcb, aux_first);
    if (PF5 && PF5P == 1) pf5_ld();
    SB0();
#pragma unroll
    for (int i = WE; i < WE + WE2; ++i) wt[PK3 + i] = ld_nt(rwl, pv1, wl_off(PK3 + i));
    SB0();
    if (w == 0) STAMP(4);
    if (w == 7) STAMP(26);
    STAMPW(7);
    {
      if (!EOS) {
        if (n < 8) {
          float* dst = redO + w * 1056 + n * 132 + g * 32;
#pragma unroll
          for (int r = 0; r < 4; ++r) {
            *(f32x4*)(dst + r * 8) = f32x4{O[0][r] * fs, O[1][r] * fs, O[2][r] * fs, O[3][r] * fs};
            *(f32x4*)(dst + r * 8 + 4) = f32x4{O[4][r] * fs, O[5][r] * fs, O[6][r] * fs, O[7][r] * fs};
          }
        }
        lds_arrive(cb, C_O);
      }
      lds_wait<0>(cv, C_O, 8);
      f32x2 s;
      if (EOS) {
        auto L = [&](int i) { return *(const lf2*)(uintptr_t)(ord + i * 4224); };
        auto T = [&](int i) { return __builtin_elementwise_fma(L(i + 1), f32x2{fw[i + 1], fw[i + 1]}, L(i) * fw[i]); };
        s = (T(0) + T(2)) + (T(4) + T(6));
      } else s = sum8x2(redO + w * 132 + lane * 2, 1056);
      unsigned ou = pk2(s[0], s[1]);
      if (T1F == 2) {
        const unsigned lo = ou & 0xffffu, u = fbits(s[1]);
        const unsigned t = __builtin_elementwise_sub_sat((u & 0x7fffffffu) + 0x8000u, lo);
        ou = (t & 0x7fff0000u) | (u & 0x80000000u) | lo;
      }
      st_sc1(canon(ou), rws, opv, ops);
    }
    SB0();
    if (PF5 && PF5P == 2) pf5_ld();
    if (w == 0) STAMP(5);
    v4u f[8];
    unsigned kw = ldsa(redU + w * RW + lane * 8), kr = ldsa(redU + w * 64 + lane), km = (lane & 8) == 0 ? ~0u : 0u;
    asm("" : "+v"(km));
    if (doT4) {
      {
        f32x4 d[8];
        const int ks = WT4 ? td >> 3 : td >> 4, c16 = WT4 ? td & 7 : td & 15;
        const rsrc_t rw = mk(a.wd, ABL & 16 ? 0u : 7u << 20);
        const int off = WT4 ? (ks * 256 + w * 32 + g * 8) * 2048 + c16 * 256 + n * 16 : (ks * 512 + w * 64 + (n >> 3) * 32 + g * 8) * 2048 + c16 * 128 + (n & 7) * 16;
        SB0();
#pragma unroll
        for (int i = 0; i < WDE; ++i) wt[i] = ld_nt(rw, off, i * 2048);
        SB0();
        if (WDG2) { lds_wait<4>(cb, C_T2, 4); if (WDG2 > 1) __builtin_amdgcn_s_sleep(WDG2); }
        else if (WDG) lds_wait<4>(cb, C_T1, WDG);
        SB0();
#pragma unroll
        for (int i = WDE; i < 8; ++i) { if (i >= 1 && i <= WDS) VMWAIT(0); else if (i >= WDE + WDS + WDW) VMWAIT(WDW - 1); SB0(); wt[i] = ld_nt(rw, off, i * 2048); SB0(); }
        SB0();
        asm volatile("" :: "v"(pf4), "v"(pf5), "v"(pf6));
        tr8((const v4u(&)[8])wt[0], f);
        PIN8(f);
        int d4v = lane * 4, d4s = WS_DP + (w * 16 + c16) * 2048 + ks * 256;
        PINV(kw); PINV(kr); PINV(km); PINV(d4v); PINS(d4s);
        if (w == 0) STAMP(12);
        lds_wait<1>(cb, C_PRE, 1);
        if (WT4) {
          v4u gu[1];
          gather<0>(gu, [&](int) { return ld_sc(rws, (n & 1) * 56 * 1024 + (n >> 1) * 128 + g * 16, WS_GU + (ks * 4 + (w >> 1)) * 1024 + (w & 1) * 64); }, miss);
          if (w == 0) STAMP(13);
          v4u hh;
#pragma unroll
          for (int m = 0; m < 4; ++m) { const unsigned y = dppu<0xB1>(gu[0][m]); hh[m] = pk2(bflo(gu[0][m]) * bflo(y), bfhi(gu[0][m]) * bfhi(y)); }
#pragma unroll
          for (int j = 0; j < 8; ++j) d[j] = mfma(hh, f[j], f32x4{0.f, 0.f, 0.f, 0.f});
          float* dst = reg + w * RW + g * 264 + n * 8;
          *(f32x4*)(dst) = f32x4{d[0][0], d[1][0], d[2][0], d[3][0]};
          *(f32x4*)(dst + 132) = f32x4{d[0][2], d[1][2], d[2][2], d[3][2]};
          *(f32x4*)(dst + 4) = f32x4{d[4][0], d[5][0], d[6][0], d[7][0]};
          *(f32x4*)(dst + 136) = f32x4{d[4][2], d[5][2], d[6][2], d[7][2]};
          lds_arrive(cb, C_T4);
          lds_wait<0>(cb, C_T4, 8);
          const f32x2 t = sum8x2(reg + w * 132 + lane * 2, RW);
          st_sc2(v2u{canon(fbits(t[0])), canon(fbits(t[1]))}, rws, (lane >> 5) * 3584 + (lane & 31) * 8, WS_DP + (w * 16 + c16 * 2) * 3584 + ks * 256);
          if (w == 0) STAMP(14);
        } else {
        v4u gu[2];
        if (GUK) ov_gather<GUK>(gu, [&](v4u& x, int i) { ov_ld(x, n * 64 + g * 16, sws, WS_GU + (ks * 8 + w) * 1024 + i * 56 * 1024); }, miss, nop);
        else gatherS<0>(gu, [&](int i) { return ld_sc(rws, n * 64 + g * 16, WS_GU + (ks * 8 + w) * 1024 + i * 56 * 1024); }, miss);
        if (w == 0) STAMP(13);
        v4u hh;
#pragma unroll
        for (int m = 0; m < 4; ++m) hh[m] = pk2(bflo(gu[0][m]) * bflo(gu[1][m]), bfhi(gu[0][m]) * bfhi(gu[1][m]));
#pragma unroll
        for (int j = 0; j < 8; ++j) d[j] = f32x4{0.f, 0.f, 0.f, 0.f};
        mm_fold(d, hh, f, km, kw);
        lds_arrive(cv, C_T4, one);
        lds_wait<0>(cv, C_T4, 8);
        const float t = sum8l(kr, RW);
        st_sc1(canon(fbits(t)), rws, d4v, d4s);
        if (w == 0) STAMP(14);
        if (GUK) ov_keep(gu);
        }
      }
    } else if (doT3) {
      auto aux_ld = [&](int i) { return ld_sc(rws, arow, aux_off(i)); };
      if (OSB) { lds_arrive(cb, C_PKD); lds_wait<0>(cb, C_PKD, 8); }
      if (OSB > 1) __builtin_amdgcn_s_sleep(OSB);
      if (SBP == 0) gat_issue(sbcb, aux_first);
#pragma unroll
      for (int i = PK3 + WE + WE2; i < 16; ++i) { VMWAIT(WGW - 1); SB0(); wt[i] = ld_nt(rwl, pv1, wl_off(i)); SB0(); }
      if (slow) {
#pragma unroll
        for (int i = PK; i < PK3; ++i) wt[i] = *(const v4u*)(pre + w * (PK3 * 256) + i * 256 + lane * 4);
      } else {
#pragma unroll
        for (int i = PK; i < PK3; ++i) { VMWAIT(WGW - 1); SB0(); wt[i] = ld_nt(rwl, pv1, wl_off(i)); SB0(); }
      }
#pragma unroll
      for (int i = 0; i < PK; ++i) wt[i] = *(const v4u*)(pre + w * (PK3 * 256) + i * 256 + lane * 4);
      v4u f1[8];
      tr8((const v4u(&)[8])wt[0], f);
      PIN8(f);
      gat_finish<1>(sbcb, aux_ld, miss);
      lds_wait<1>(cb, C_M2, 4);
      SB0();
      tr8((const v4u(&)[8])wt[8], f1);
      PIN8(f1);
      if (w == 0) STAMP(31);
      SB0();
      f32x4 d[8];
#pragma unroll
      for (int j = 0; j < 8; ++j) d[j] = mfma(sbcb[1], f1[j], mfma(sbcb[0], f[j], f32x4{0.f, 0.f, 0.f, 0.f}));
      kfold_store(d, redT + w * RW, lane);
#pragma unroll
      for (int j = 0; j < 8; ++j) d[j] = mfma(sbcb[3], f1[j], mfma(sbcb[2], f[j], f32x4{0.f, 0.f, 0.f, 0.f}));
      kfold_store(d, redT + 512 + w * RW, lane);
      lds_arrive(cb, C_C12);
      float c1, c2;
      auto c12 = [&]() {
        lds_wait<1>(cb, C_C12, 8);
        c2 = sum8(redT + w * 64 + lane, RW); c1 = sum8(redT + 512 + w * 64 + lane, RW);
        asm volatile("" : "+v"(c1), "+v"(c2));
        if (w == 0) STAMP(9);
      };
      float neg = ta < 56 ? 0.f : 1.f;
      unsigned sta = ldsa(mlw + w * 2);
      PINV(neg); PINV(sta);
      int g3v = (lane >> 1) * 4, g3s = WS_GU + ta * 1024 + w * 128;
      PINV(g3v); PINS(g3s); PINV(kw); PINV(kr); PINV(km);
      if (!DC12) c12();
      lds_wait<1>(cb, C_PRE, 1);
      v4u xr[2];
      {
        const int sb0 = WS_X1 + xc * REPX;
        auto x_ld = [&](int i) { return ld_sc(rws, arow, sb0 + w * 256 + i * 128); };
        if (X1K) ov_gather<X1K>(xr, [&](v4u& x, int i) { ov_ld(x, arow, sws, sb0 + w * 256 + i * 128); }, miss, c12);
        else {
          gat_issue(xr, x_ld);
          if (DC12) c12();
          SB0();
          fin<0>(xr, x_ld);
        }
      }
      if (w == 0) STAMP(10);
#pragma unroll
      for (int j = 0; j < 8; ++j) d[j] = mfma(xr[0], f[j], f32x4{0.f, 0.f, 0.f, 0.f});
      mm_fold(d, xr[1], f1, km, kw);
      lds_arrive(cv, C_T3, one);
      lds_wait<0>(cv, C_T3, 9);
      const f32x2 st = *(const lf2*)(uintptr_t)sta;
      float t = fmaf(fmaf(st[0], c1, sum8l(kr, RW)), st[1], c2);
      t = t > 0.f ? t : t * neg;
      st_sc1(canon(pk2(dppf<0xA0>(t), dppf<0xF5>(t))), rws, g3v, g3s);
      if (w == 0) STAMP(11);
      if (X1K) ov_keep(xr);
    } else if (PF6) asm volatile("" :: "v"(pf5), "v"(pf6));
  } else {
    const int cw = w - 8;
    unsigned tdiag = 0;
    __builtin_amdgcn_s_setprio(2);
    const v2u epv = __builtin_amdgcn_raw_buffer_load_b64(rws, 0, WS_EPOCH, 0);
    SB0();
    unsigned tlo, thi;
    auto mktag = [&]() {
      const u64 tag = (((u64)__builtin_amdgcn_readfirstlane(epv.y) << 32) | __builtin_amdgcn_readfirstlane(epv.x)) + 0x9E3779B97F4A7C15ull;
      tlo = (unsigned)tag; thi = (unsigned)(tag >> 32);
      asm volatile("" : "+s"(tlo), "+s"(thi) : "v"(pfc));
      asm volatile("s_waitcnt lgkmcnt(0)\n\ts_barrier" ::: "memory");
    };
    const rsrc_t rc = mk(a.c, 16384), rm = mk(a.wmod, 12u << 20), rx = mk(a.x, 16384);
    const int wlane_r = wl, wlane_c = wc;
    if (__builtin_expect(cw >= 2, 1)) {
      const int h = cw - 2;
      const int ct = DEC ? ((jx + 3) & 7) + (jx & 24) : bid & 31, ks = DEC ? xc : bid >> 5;
      const int qct = DEC ? ((jx + 4) & 7) + 8 * xc : bid & 63, qks = bid >> 6;
      v4u wm[8], wq0[8], wq1[8];
      const v4u cv = ld_pl(rc, arow, (ks * 128 + h * 64) * 2);
      {
        const int off = (ks * 128 + h * 64 + wlane_r) * 12288 + ct * 128 + wlane_c;
#pragma unroll
        for (int i = 0; i < 8; ++i) wm[i] = ld_nt(rm, off, i * 12288);
      }
      SB0();
      auto wq_ld = [&]() {
        const rsrc_t rq = mk(a.wq, 8u << 20);
        const int off = (qks * 256 + h * 128 + wlane_r) * 8192 + qct * 128 + wlane_c;
#pragma unroll
        for (int i = 0; i < 8; ++i) wq0[i] = ld_nt(rq, off, i * 8192);
#pragma unroll
        for (int i = 0; i < 8; ++i) wq1[i] = ld_nt(rq, off, (64 + i) * 8192);
      };
      wq_ld();
      SB0();
      SB0();
      mktag();
      auto tmiss = [&](v4u x) { return ((x.y ^ tlo) | (x.w ^ thi)) != 0; };
      const v4u ca = silu8(cv);
      v4u f[8];
      f32x4 d[8];
      tr8(wm, f);
#pragma unroll
      for (int j = 0; j < 8; ++j) d[j] = mfma(ca, f[j], f32x4{0.f, 0.f, 0.f, 0.f});
      kfold_store(d, redC + h * 512, lane);
      lds_arrive(cb, C_M1);
      lds_wait<0>(cb, C_M1, 2);
      {
        const float* p = redC + h * 256 + lane * 2;
        const f32x2 s0 = *(const f32x2*)p + *(const f32x2*)(p + 512), s1 = *(const f32x2*)(p + 128) + *(const f32x2*)(p + 640);
        st_sc(v4u{fbits(s0[0]), tlo, fbits(s0[1]), thi}, rws, lane * 16, WS_MPA + (ct * 8 + ks) * MPA_SLOT + h * 2048);
        st_sc(v4u{fbits(s1[0]), tlo, fbits(s1[1]), thi}, rws, lane * 16, WS_MPA + (ct * 8 + ks) * MPA_SLOT + h * 2048 + 1024);
      }
      lds_arrive(cb, C_M1S);
      if (cw == 2) STAMP(16);
      SB0();
      v4u f2[8];
      tr8(wq0, f);
      tr8(wq1, f2);
      PIN8(f);
      PIN8(f2);
      if (cw == 2) STAMP(28);
      lds_wait<1>(cb, C_R1, 1);
      if (G2) pace(tstart, G2);
      v4u ha[4];
      gather<1>(ha, [&](int i) { return ld_sc(rws, (n >> 1) * 4096 + (n & 1) * 64 + g * 16, WS_H + xc * HREP + (qks * 256 + h * 128) * 4 + i * 128); }, tmiss);
      if (cw == 2) STAMP(19);
      const v4u a0 = {ha[0].x, ha[0].z, ha[1].x, ha[1].z}, a1 = {ha[2].x, ha[2].z, ha[3].x, ha[3].z};
#pragma unroll
      for (int j = 0; j < 8; ++j) d[j] = mfma(a1, f2[j], mfma(a0, f[j], f32x4{0.f, 0.f, 0.f, 0.f}));
      kfold_store(d, redC + 1024 + h * 512, lane);
      lds_arrive(cb, C_Q);
      lds_wait<0>(cb, C_Q, 2);
      {
        f32x4 s = *(const f32x4*)(redC + 1024 + h * 256 + lane * 4) + *(const f32x4*)(redC + 1536 + h * 256 + lane * 4);
        lds_wait<1>(cb, C_GO, 1);
        st_sc(canon4(s), rws, g * 65536 + n * 16, WS_QP + h * 4 * 65536 + (qct >> 4) * 16384 + qks * 4096 + (qct & 15) * 256);
      }
      lds_arrive(cb, C_QS);
      if (cw == 2) STAMP(20);
    } else {
      v4u xr[2];
      unsigned xp = 0;
      if (PREF == 1 && cw == 0) {
        u64 pc;
        unsigned len;
        asm volatile("s_getpc_b64 %0\n.Lref%=:\n\ts_mov_b32 %1, .Lref%=-_Z4mega4Args" : "=s"(pc), "=s"(len));
        pc -= len;
        asm volatile("s_mov_b32 %0, _Z8mega_endv-_Z4mega4Args" : "=s"(len));
        const rsrc_t rcode = mk((const void*)pc, len);
        xr[0] = ld_pl(rcode, lane * 16, jx * 1024); xr[1] = ld_pl(rcode, lane * 16, jx * 1024 + 32768);
      }
      if (cw == 1) {
        xr[0] = ld_pl(rx, lane * 16, xc * 2048); xr[1] = ld_pl(rx, lane * 16, xc * 2048 + 1024);
        xp = ld_pl1(rx, n * 4, xc * 2048 + jx * 64);
      }
      SB0();
      mktag();
      if (cw == 0) {
        lds_wait<1>(cb, C_RESET, 8);
        if (PREF == 1) asm volatile("" :: "v"(xr[0]), "v"(xr[1]));
        if (lane < 8) st_sc2(v2u{tlo, thi}, rws, lane * FLAG_REP, WS_FLAGS + bid * 8);
        STAMP(15);
        lds_wait<4>(cb, C_R1, 1);
        if (G3) pace(tstart, G3);
        v4u fl[2];
        gather<4>(fl, [&](int i) { return ld_sc(rws, lane * 16, WS_FLAGS + xc * FLAG_REP + i * 1024); },
                  [&](v4u x) { return ((x.x ^ tlo) | (x.y ^ thi) | (x.z ^ tlo) | (x.w ^ thi)) != 0; });
        lds_arrive(cb, C_GO);
        if (bid == 0 && lane == 0) st_sc2(v2u{tlo, thi}, rws, 0, WS_EPOCH);
        STAMP(17);
        if (TV) { const unsigned t = ld_pl1(mk(a.vc, 64u << 20), (lane & (TV - 1)) * ((256 << 10) / (TV ? TV : 1)), (grp * 8 + as) * (256 << 10)); asm volatile("" :: "v"(t)); }
      } else {
        float ln0, ln1;
        {
          const float x0 = bitsf(__builtin_amdgcn_readfirstlane(xr[0][0] << 16));
          float s = 0.f, q = 0.f;
#pragma unroll
          for (int i = 0; i < 8; ++i) {
            const float d0 = bflo(xr[i >> 2][i & 3]) - x0, d1 = bfhi(xr[i >> 2][i & 3]) - x0;
            s += d0 + d1; q = fmaf(d0, d0, fmaf(d1, d1, q));
          }
          s = wave_sum(s); q = wave_sum(q);
          const float md = s * (1.f / 1024.f);
          const float var = fmaxf(q * (1.f / 1024.f) - md * md, 0.f);
          const float mean = rbf(x0 + md);
          const float den = rbf(__builtin_sqrtf(rbf(rbf(var) + 1e-5f)));
          ln0 = rbf(rbf(bflo(xp) - mean) / den); ln1 = rbf(rbf(bfhi(xp) - mean) / den);
          asm volatile("" : "+v"(ln0), "+v"(ln1));
        }
        lds_wait<1>(cb, C_M1S, 2);
        if (G1) pace(tstart, G1);
        v4u mp[4];
        gather<1>(mp, [&](int i) { return ld_sc(rws, g * MPA_SLOT + n * 16, WS_MPA + (jx >> 1) * 8 * MPA_SLOT + xc * 512 + (jx & 1) * 256 + (i >> 1) * 128 * MPA_SLOT + (i & 1) * 4 * MPA_SLOT); },
                  [&](v4u x) { return ((x.y ^ tlo) | (x.w ^ thi)) != 0; });
        const float sa0 = rbf(add32(add16(bitsf(mp[0].x) + bitsf(mp[1].x)))), sa1 = rbf(add32(add16(bitsf(mp[0].z) + bitsf(mp[1].z))));
        const float ca0 = rbf(add32(add16(bitsf(mp[2].x) + bitsf(mp[3].x)))), ca1 = rbf(add32(add16(bitsf(mp[2].z) + bitsf(mp[3].z))));
        const unsigned u = pk2(rbf(ln0 * rbf(1.f + ca0)) + sa0, rbf(ln1 * rbf(1.f + ca1)) + sa1);
        st_sc(v4u{dppu<0xA0>(u), tlo, dppu<0xF5>(u), thi}, rws, (g * 2 + (n & 1)) * HREP + ((n >> 1) & 1) * 128 + (n >> 2) * 16, WS_H + xc * 4096 + (jx >> 1) * 256 + (jx & 1) * 64);
        lds_arrive(cb, C_R1);
        STAMP(18);
      }
    }
    lds_wait<1>(cb, C_QS, 2);
    if (G4) pace(tstart, G4);
    {
      v4u qp[4];
      gather<1>(qp, [&](int i) { return ld_sc(rws, lane * 16, WS_QP + grp * 16384 + cw * 1024 + i * 4096); }, miss);
      f32x4 s = (asf(qp[0]) + asf(qp[1])) + (asf(qp[2]) + asf(qp[3]));
      *(v2u*)(qs + (cw * 2 + (lane >> 5)) * 68 + (lane & 31) * 2) = v2u{pk2(s[0], s[1]), pk2(s[2], s[3])};
      lds_arrive(cb, C_QG);
      if (cw == 0) STAMP(21);
      if (DIAG == 2) tdiag = now();
    }
    const bool red = (jx & 14) == 14;
    const int cblk = ((jx >> 4) * 2 + (jx & 1)) * 4 + cw, unit = xc * 16 + cblk, cp = lane & 31, half = lane >> 5;
    const int cpp = GPM ? (cp & 7) * 4 + (cp >> 3) : cp;
    auto add2 = [](v2u p, v2u q) { return f32x2{bitsf(p.x) + bitsf(q.x), bitsf(p.y) + bitsf(q.y)}; };
    auto a32 = [](f32x2 s) { return f32x2{add32(s[0]), add32(s[1])}; };
    v4u f0[8], f1[8];
    __builtin_amdgcn_s_setprio(0);
    {
      const int ct = m2c, ks = jx >> 3;
      const v4u c0 = ld_pl(rc, arow, (ks & 1) * 1024 + cw * 256), c1 = ld_pl(rc, arow, (ks & 1) * 1024 + cw * 256 + 128);
      lds_wait<2>(cb, C_PARK, 8);
      const float* pk = reg + 2 * cw * RW + lane * 4;
      v4u w0[8], w1[8];
#pragma unroll
      for (int i = 0; i < 8; ++i) { w0[i] = *(const v4u*)(pk + i * 256); w1[i] = *(const v4u*)(pk + RW + i * 256); }
      tr8(w0, f0);
      PIN8(f0);
      tr8(w1, f1);
      PIN8(f1);
      if (!typeB) {
        const v4u a0 = silu8(c0), a1 = silu8(c1);
        f32x4 d[8];
#pragma unroll
        for (int j = 0; j < 8; ++j) d[j] = mfma(a1, f1[j], mfma(a0, f0[j], f32x4{0.f, 0.f, 0.f, 0.f}));
        kfold_store(d, redC + 2048 + cw * 512, lane);
        lds_arrive(cb, C_M2);
        lds_wait<0>(cb, C_M2, 4);
        const float* src = redC + 2048 + (lane >> 5) * 1024 + cw * 128 + (lane & 31) * 4;
        f32x4 s = *(const f32x4*)src + *(const f32x4*)(src + 512);
        s = f32x4{add32(s[0]), add32(s[1]), add32(s[2]), add32(s[3])};
        if (lane < 32) st_sc(canon4(s), rws, g * 32768 + n * 16, WS_MPB + cw * 65536 + (ct & 15) * 2048 + (ct >> 4) * 512 + ks * 256);
        if (cw == 0) STAMP(22);
      }
    }
    if (PF3 && cw == 3) {
      const int ob = jx < 16 ? 16 + xc : (xc >= 6 ? 8 + xc : 24 + xc), ot = (ob % 14) * 8 + 3;
      const rsrc_t rp = mk(ot >= 56 ? a.wu : a.wg, ob < 28 ? 7u << 20 : 0u);
      const int row = PF3 == 1 ? (jx & 15) * 64 + lane : (jx & 14) * 64 + (jx & 1) * 32 + (lane & 31) + (PF3 == 2 ? 64 : 0);
      pace(tstart, GP);
      const unsigned t = __builtin_amdgcn_raw_buffer_load_b32(rp, row * 7168, (ot % 56) * 128, PFA);
      asm volatile("" :: "v"(t));
    }
    if (TW && cw == 2) {
      const int td = xc * 14 + jb;
      const rsrc_t rp = mk(typeB ? a.wd : ta >= 56 ? a.wu : a.wg, doT3 || doT4 ? 7u << 20 : 0u);
      pace(tstart, GT);
      if (lane < TW) {
        const unsigned t = ld_pl1(rp, typeB ? lane * (512 / (TW ? TW : 1)) * 2048 : lane * (1024 / (TW ? TW : 1)) * 7168, typeB ? (td >> 4) * (1 << 20) + (td & 15) * 128 : (ta % 56) * 128);
        asm volatile("" :: "v"(t));
      }
    }
    unsigned xo = 0;
    f32x2 ga, gb, cb1;
    if (red) {
      if (TO) { const unsigned t = ld_pl1(mk(a.out, 16384), cp * 4, xc * 2048 + cblk * 128); asm volatile("" :: "v"(t)); }
      xo = ld_pl1(rx, cp * 4, xc * 2048 + cblk * 128);
      v2u mp[4];
      if (G5) pace(tstart, G5);
      gather<8>(mp, [&](int j) { return ld_sc2(rws, lane * 8, WS_MPB + unit * 2048 + j * 512); }, miss);
      auto h2 = [&](v2u p) { return a32(f32x2{bitsf(p.x), bitsf(p.y)}); };
      ga = h2(mp[0]); ga = f32x2{rbf(ga[0]), rbf(ga[1])};
      const f32x2 sb = h2(mp[1]);
      cb1 = h2(mp[2]); cb1 = f32x2{rbf(1.f + rbf(cb1[0])), rbf(1.f + rbf(cb1[1]))};
      gb = h2(mp[3]); gb = f32x2{rbf(gb[0]), rbf(gb[1])};
      const f32x2 z = half ? cb1 : sb;
      st_sc1(canon(pk2(z[0], z[1])), rws, half * 16384 + cpp * 4, WS_SB + xc * 2048 + cblk * 128);
      if (cw == 0) STAMP(24);
    }
    if (typeB) {
      const int hp = 2 * xc + (jb >> 3), cg = jb & 7, head = 2 * hp + (cw >> 1);
      if (cw == 0) STAMP(27);
      f32x4 F;
      {
        lds_wait<8>(cb, C_MLF, 1);
        v4u ml[1];
        gather<8>(ml, [&](int) { return ld_sc(rws, (n >> 1) * 64 + g * 16, WS_ML + head * 512); }, miss);
        const float M = max32(max16(fmaxf(bitsf(ml[0].x), bitsf(ml[0].z))));
        const float w0 = bitsf(ml[0].y) * ex2((bitsf(ml[0].x) - M) * LOG2E), w1 = bitsf(ml[0].w) * ex2((bitsf(ml[0].z) - M) * LOG2E);
        const float mine = ((lane & 1) ? w1 : w0) * rcp(add32(add16(w0 + w1)));
        const auto s = __builtin_amdgcn_permlane16_swap(fbits(mine), fbits(mine), false, false);
        const auto e = __builtin_amdgcn_permlane32_swap(s[0], s[0], false, false), o = __builtin_amdgcn_permlane32_swap(s[1], s[1], false, false);
        F = f32x4{bitsf(e[0]), bitsf(o[0]), bitsf(e[1]), bitsf(o[1])};
      }
      asm volatile("" : "+v"(F));
      unsigned t1w = ldsa(redC + cw * 1056 + g * 264 + n * 8), t1r = ldsa(redC + (2 * cw + (lane >> 5)) * 132 + (lane & 31) * 4);
      int t1v = (lane >> 5) * 65536 + (g & 1) * 4096 + n * 16, t1s = WS_XP + (cw * 32 + cg * 2) * 4096 + hp * 256;
      unsigned cv = cb, one = 1u;
      if (T1F) asm volatile("" : "+v"(t1w), "+v"(t1r), "+v"(t1v), "+s"(t1s));
      if (HOI > 1) asm volatile("" : "+v"(cv), "+v"(one));
      __builtin_amdgcn_s_setprio(2);
      lds_wait<2>(cb, C_O, 8);
      if (OPD) __builtin_amdgcn_s_sleep(OPD);
      if (G6) pace(tstart, PA + (NS * TAU) / 16 + G6);
      v4u oa[8];
      if (OPK) ov_gather<OPK>(oa, [&](v4u& x, int q) { ov_ld(x, n * 64 + g * 16, sws, WS_OP + head * 16384 + (cw & 1) * 8192 + q * 1024); }, miss, nop);
      else gatherS<0>(oa, [&](int q) { return ld_sc(rws, n * 64 + g * 16, WS_OP + head * 16384 + (cw & 1) * 8192 + q * 1024); }, miss);
      if (cw == 0) STAMP(6);
      if (DIAG == 5) tdiag = now();
      if (T1F) {
        typedef __attribute__((address_space(3))) f32x4 lf4;
        auto hi = [](unsigned x) { return T1F == 2 ? bitsf(x) : bfhi(x); };
        auto merge = [&](int j, int m, v4u& o) {
          f32x2 l = f32x2{bflo(oa[4 * j][m]), bflo(oa[4 * j][m + 1])} * F[0], h = f32x2{hi(oa[4 * j][m]), hi(oa[4 * j][m + 1])} * F[0];
#pragma unroll
          for (int p = 1; p < 4; ++p) {
            l = __builtin_elementwise_fma(f32x2{bflo(oa[4 * j + p][m]), bflo(oa[4 * j + p][m + 1])}, f32x2{F[p], F[p]}, l);
            h = __builtin_elementwise_fma(f32x2{hi(oa[4 * j + p][m]), hi(oa[4 * j + p][m + 1])}, f32x2{F[p], F[p]}, h);
          }
          auto sw = [](float x) { x += dppf<0xB1>(x); asm("" : "+v"(x)); return x; };
          o[m] = pk2(sw(l[0]), sw(h[0])); o[m + 1] = pk2(sw(l[1]), sw(h[1]));
        };
        v4u o0, o1;
        f32x4 d[8];
        merge(0, 0, o0); merge(0, 2, o0);
        SB0();
#pragma unroll
        for (int j = 0; j < 8; ++j) d[j] = mfma(o0, f0[j], f32x4{0.f, 0.f, 0.f, 0.f});
        merge(1, 0, o1); merge(1, 2, o1);
#pragma unroll
        for (int j = 0; j < 8; ++j) {
          __builtin_amdgcn_sched_group_barrier(0x008, 1, 0);
          __builtin_amdgcn_sched_group_barrier(0x002, T1F == 2 ? 6 : 8, 0);
        }
        SB0();
#pragma unroll
        for (int j = 0; j < 8; ++j) d[j] = mfma(o1, f1[j], d[j]);
        auto row = [&](int j, int r) { return f32x4{d[j][r], d[j + 1][r], d[j + 2][r], d[j + 3][r]}; };
        *(lf4*)(uintptr_t)(t1w) = row(0, 0);
        *(lf4*)(uintptr_t)(t1w + 528) = row(0, 2);
        *(lf4*)(uintptr_t)(t1w + 16) = row(4, 0);
        *(lf4*)(uintptr_t)(t1w + 544) = row(4, 2);
        lds_arrive(cv, C_T1, one);
        lds_wait<0>(cv, C_T1, 4);
        auto L = [&](int i) { return *(const lf4*)(uintptr_t)(t1r + i * 4224); };
        st_sc(canon4((L(0) + L(1)) + (L(2) + L(3))), rws, t1v, t1s);
      } else {
      v4u oo[2];
#pragma unroll
      for (int j = 0; j < 2; ++j)
#pragma unroll
        for (int m = 0; m < 4; ++m) {
          float lo = bflo(oa[4 * j][m]) * F[0], hi = bfhi(oa[4 * j][m]) * F[0];
#pragma unroll
          for (int p = 1; p < 4; ++p) { lo = fmaf(bflo(oa[4 * j + p][m]), F[p], lo); hi = fmaf(bfhi(oa[4 * j + p][m]), F[p], hi); }
          oo[j][m] = pk2(lo + dppf<0xB1>(lo), hi + dppf<0xB1>(hi));
        }
      f32x4 d[8];
      float* dst = redC + cw * 1056 + g * 264 + n * 8;
#pragma unroll
      for (int j = 0; j < 8; ++j) d[j] = mfma(oo[1], f1[j], mfma(oo[0], f0[j], f32x4{0.f, 0.f, 0.f, 0.f}));
      *(f32x4*)(dst) = f32x4{d[0][0], d[1][0], d[2][0], d[3][0]};
      *(f32x4*)(dst + 132) = f32x4{d[0][2], d[1][2], d[2][2], d[3][2]};
      *(f32x4*)(dst + 4) = f32x4{d[4][0], d[5][0], d[6][0], d[7][0]};
      *(f32x4*)(dst + 136) = f32x4{d[4][2], d[5][2], d[6][2], d[7][2]};
      lds_arrive(cb, C_T1);
      lds_wait<0>(cb, C_T1, 4);
      {
        const float* src = redC + (2 * cw + (lane >> 5)) * 132 + (lane & 31) * 4;
        const f32x4 s = (*(const f32x4*)src + *(const f32x4*)(src + 1056)) + (*(const f32x4*)(src + 2112) + *(const f32x4*)(src + 3168));
        st_sc(canon4(s), rws, (lane >> 5) * 65536 + (g & 1) * 4096 + n * 16, WS_XP + (cw * 32 + cg * 2) * 4096 + hp * 256);
      }
      }
      if (WDG2) lds_arrive(cb, C_T2);
      if (cw == 0) STAMP(7);
      if (OPK) ov_keep(oa);
    }
    if (doT3 && cw == 0) {
      lds_wait<8>(cb, C_O, 8);
      if (PRW) __builtin_amdgcn_s_sleep(PRW);
      unsigned t[1];
      if (PRK) ov_gather<PRK>(t, [&](unsigned& x, int) { ov_ld(x, n * 256, sws, WS_XP + (xc * 16 + jx) * 4096); }, miss, nop);
      else gather<PRS>(t, [&](int) { return ld_sc1(rws, n * 256, WS_XP + (xc * 16 + jx) * 4096); }, miss);
      if (PRD) __builtin_amdgcn_s_sleep(PRD);
      lds_arrive(cb, C_PRE);
      STAMP(29);
      __builtin_amdgcn_s_setprio(2);
      if (STD) __builtin_amdgcn_s_sleep(STD);
      v4u q[1];
      if (STK) ov_gather<STK>(q, [&](v4u& x, int) { ov_ld(x, lane * 16, sws, WS_X1 + xc * REPX + 16384); }, miss, nop);
      else gather<STS>(q, [&](int) { return ld_sc(rws, lane * 16, WS_X1 + xc * REPX + 16384); }, miss);
      auto s8 = [](float x) { x += dppf<0xB1>(x); x += dppf<0x4E>(x); return x + dppf<0x141>(x); };
      const float m0 = bitsf(q[0].x), m1 = bitsf(q[0].z);
      const float mu = s8(m0 + m1) * (1.f / 16.f);
      const float var = s8(fmaf((m0 - mu) * 64.f, m0 - mu, bitsf(q[0].y)) + fmaf((m1 - mu) * 64.f, m1 - mu, bitsf(q[0].w))) * (1.f / 1024.f);
      *(f32x2*)(mlw + (lane >> 3) * 2) = f32x2{-rbf(mu), rcp(rbf(__builtin_amdgcn_sqrtf(rbf(rbf(var) + 1e-5f))))};
      lds_arrive(cb, C_T3);
      STAMP(30);
      if (PRK) ov_keep(t);
      if (STK) ov_keep(q);
    }
    if (doT4 && cw == 0) {
      unsigned t[1];
      if (PRW4) __builtin_amdgcn_s_sleep(PRW4);
      if (PR4K) ov_gather<PR4K>(t, [&](unsigned& x, int) { ov_ld(x, (lane & 7) * 2048, sws, WS_X1 + xc * REPX + jb * 128); }, miss, nop);
      else gather<4>(t, [&](int) { return ld_sc1(rws, (lane & 7) * 2048, WS_X1 + xc * REPX + jb * 128); }, miss);
      if (PRD4) __builtin_amdgcn_s_sleep(PRD4);
      lds_arrive(cb, C_PRE);
      STAMP(23);
      if (PR4K) ov_keep(t);
    }
    if (red) {
      __builtin_amdgcn_s_setprio(2);
      lds_wait<4>(cb, C_O, 8);
      if (DIAG == 3) tdiag = typeB ? tstart : now();
      f32x2 x1;
      {
        v2u xp[8];
        int x1v = half * REPX + cpp * 4, x1s[4], stv = lane * REPX, sts = WS_X1 + 16384 + xc * 128 + cblk * 8;
        f32x2 xf = {bflo(xo), bfhi(xo)};
#pragma unroll
        for (int r = 0; r < 4; ++r) { x1s[r] = WS_X1 + 2 * r * REPX + xc * 2048 + cblk * 128; PINS(x1s[r]); }
        PINV(x1v); PINV(stv); PINS(sts); PINV(xf);
        if (XPS && !typeB) __builtin_amdgcn_s_sleep(XPS);
        if (XPB && typeB) __builtin_amdgcn_s_sleep(XPB);
        if (XPK) ov_gather<XPK>(xp, [&](v2u& x, int j) { ov_ld(x, half * 2048 + cp * 8, sws, WS_XP + unit * 4096 + j * 256); }, miss, nop);
        else gatherS<1>(xp, [&](int j) { return ld_sc2(rws, half * 2048 + cp * 8, WS_XP + unit * 4096 + j * 256); }, miss);
        const f32x2 s = a32((add2(xp[0], xp[1]) + add2(xp[2], xp[3])) + (add2(xp[4], xp[5]) + add2(xp[6], xp[7])));
        x1 = f32x2{rbf(xf[0] + rbf(ga[0] * rbf(s[0]))), rbf(xf[1] + rbf(ga[1] * rbf(s[1])))};
        const unsigned u = canon(pk2(x1[0] * cb1[0], x1[1] * cb1[1]));
#pragma unroll
        for (int r = 0; r < 4; ++r) st_sc1(u, rws, x1v, x1s[r]);
        const float x0 = bitsf(__builtin_amdgcn_readfirstlane(fbits(x1[0])));
        const float e0 = x1[0] - x0, e1 = x1[1] - x0;
        const float sm = sum32(e0 + e1), sq = sum32(fmaf(e0, e0, e1 * e1));
        const float md = sm * (1.f / 64.f);
        if (lane < 8) st_sc2(v2u{canon(fbits(x0 + md)), canon(fbits(fmaxf(sq - sm * md, 0.f)))}, rws, stv, sts);
        if (cw == 0) STAMP(8);
        if (DIAG == 4) tdiag = now();
        if (XPK) ov_keep(xp);
      }
      rsrc_t rout = mk(a.out, 16384);
      unsigned lm = half ? 0u : ~0u;
      if (HOI) asm volatile("" : "+s"(rout), "+v"(lm));
      __builtin_amdgcn_s_sleep(DPS);
      {
        v2u dp[WT4 ? 7 : 4];
        f32x2 s;
        if (WT4) {
          gather<1>(dp, [&](int j) { return ld_sc2(rws, half * 1792 + cp * 8, WS_DP + unit * 3584 + j * 256); }, miss);
          s = a32((add2(dp[0], dp[1]) + add2(dp[2], dp[3])) + (add2(dp[4 % (WT4 ? 7 : 4)], dp[5 % (WT4 ? 7 : 4)]) + f32x2{bitsf(dp[6 % (WT4 ? 7 : 4)].x), bitsf(dp[6 % (WT4 ? 7 : 4)].y)}));
        } else {
        if (DPK) ov_gather<DPK>(dp, [&](v2u& x, int j) { ov_ld(x, half * (j < 3 ? 1024 : 768) + cp * 8, sws, WS_DP + unit * 2048 + j * 256); }, miss, nop);
        else gatherS<1>(dp, [&](int j) { return j < 3 ? ld_sc2(rws, half * 1024 + cp * 8, WS_DP + unit * 2048 + j * 256) : ld_sc2(rws, half * 768 + cp * 8, WS_DP + unit * 2048 + 768); }, miss);
        const v2u d3 = dp[3] & lm;
        s = a32(add2(dp[0], dp[1]) + add2(dp[2], d3));
        }
        const unsigned u = pk2(x1[0] + rbf(gb[0] * rbf(s[0])), x1[1] + rbf(gb[1] * rbf(s[1])));
        __builtin_amdgcn_raw_buffer_store_b32(u, rout, cp * 4, xc * 2048 + cblk * 128, 0);
        if (cw == 0) STAMP(25);
        if (DIAG) {
          const unsigned te = now();
          if (DIAG == 1) tdiag = te;
          if (DIAG == 5 && !typeB) tdiag = tstart;
          unsigned due = 2 * (tdiag - tstart);
          if (due > 20000) due = 20000;
#pragma unroll 1
          for (int it = 0; it < 8192; ++it) {
            if ((int)(now() - te) >= (int)due) break;
            __builtin_amdgcn_s_sleep(2);
          }
        }
        if (DPK) ov_keep(dp);
      }
    }
  }
}

__global__ void mega_end() {}

extern "C" void kernel_launch(void* const* d_in, const int* in_sizes, int n_in, void* d_out, int out_size, void* d_ws, size_t ws_size, hipStream_t stream) {
  Args a;
  a.x = (const u16*)d_in[0]; a.kc = (const u16*)d_in[1]; a.vc = (const u16*)d_in[2]; a.wq = (const u16*)d_in[3]; a.wo = (const u16*)d_in[4];
  a.wg = (const u16*)d_in[6]; a.wu = (const u16*)d_in[7]; a.wd = (const u16*)d_in[8]; a.c = (const u16*)d_in[9]; a.wmod = (const u16*)d_in[10];
  a.out = (u16*)d_out; a.ws = (char*)d_ws + WSOFF;
  mega<<<256, 768, 0, stream>>>(a);
}
```
